```python
import torch

def _softmax(x, axis=-1):
    return torch.softmax(x, dim=axis)
def _gelu(x):
    return torch.nn.functional.gelu(x, approximate="tanh")
def _silu(x):
    return torch.nn.functional.silu(x)
def _sigmoid(x):
    return torch.sigmoid(x)
def _layernorm(x, axis=-1, eps=1e-5):
    m = x.mean(dim=axis, keepdim=True)
    v = x.var(dim=axis, keepdim=True, unbiased=False)
    return (x - m) / torch.sqrt(v + eps)
def _scatter_add(tgt, idx, src, axis=0):
    return tgt.clone().index_add_(axis, idx, src)
def _segment_sum(src, seg, n):
    out = torch.zeros((n,) + src.shape[1:], dtype=src.dtype)
    return out.index_add_(0, seg, src)
def _pad(x, pw):
    flat = tuple(v for pair in reversed(pw) for v in pair)
    return torch.nn.functional.pad(x, flat)

kernel_name = 'standalone__molecular_angular_gemnet__graph'

def setup_inputs():

    _w = (lambda a: (lambda t: t.to(torch.bfloat16) if t.is_floating_point() else t)(torch.as_tensor(a)))
    N, deg, De, L = 50000, 16, 4, 2
    E = N * deg
    T = E * deg
    NR = 8
    dist = torch.rand(E).to(float)
    angle = (torch.rand(T) * torch.pi).to(float)
    centers = torch.linspace(0.0, 1.0, NR).to(float)
    ang_freq = torch.arange(1, L + 1).to(float)
    return {
        "m": _w((lambda *s: torch.randn(*s, dtype=torch.bfloat16) * 0.05)(E, De)),
        "idx_in": _w((lambda lo, hi, *s: torch.randint(lo, hi, s))(0, E, T).to(torch.int64)),
        "idx_out": _w((lambda lo, hi, *s: torch.randint(lo, hi, s))(0, E, T).to(torch.int64)),
        "dist": _w(dist),
        "angle": _w(angle),
        "centers": _w(centers),
        "ang_freq": _w(ang_freq),
        "w_rbf": _w((lambda *s: torch.randn(*s, dtype=torch.bfloat16) * 0.2)(NR, De)),
        "w_sbf": _w((lambda *s: torch.randn(*s, dtype=torch.bfloat16) * 0.2)(NR * L, De)),
        "w_down": _w((lambda *s: torch.randn(*s, dtype=torch.bfloat16) * 0.2)(De, De)),
        "w_up": _w((lambda *s: torch.randn(*s, dtype=torch.bfloat16) * 0.2)(De, De)),
    }

def reference(m, idx_in, idx_out, dist, angle, centers, ang_freq, w_rbf, w_sbf, w_down, w_up):
    E, De = m.shape[0], m.shape[1]
    NR = centers.shape[0]
    L = ang_freq.shape[0]
    rbf = torch.exp(-10.0 * (dist[:, None] - centers[None, :]) ** 2)
    cbf = torch.cos(ang_freq[None, :] * angle[:, None])
    rbf_t = rbf[idx_out]
    sbf = torch.reshape(rbf_t[:, :, None] * cbf[:, None, :], (-1, NR * L))
    x = (m[idx_in] @ w_down) * (sbf @ w_sbf)
    agg = _segment_sum(x, idx_out, E)
    return m + (agg @ w_up) * (rbf @ w_rbf)
```

```cpp
#include <hip/hip_runtime.h>
#include <hip/hip_bf16.h>
#include <stdint.h>

typedef unsigned short u16;
__device__ __forceinline__ float bf2f(u16 x) { return __uint_as_float(((unsigned)x) << 16); }
__device__ __forceinline__ u16 f2bf(float f) {
    unsigned u = __float_as_uint(f);
    return (u16)((u + 0x7FFF + ((u >> 16) & 1)) >> 16);
}

__global__ void k_zero(float* A, int n) {
    int i = blockIdx.x * blockDim.x + threadIdx.x;
    if (i < n) A[i] = 0.f;
}

__global__ void k_trip(const u16* __restrict__ m, const int* __restrict__ idx_in, const int* __restrict__ idx_out,
                       const u16* __restrict__ angle, const u16* __restrict__ ang_freq,
                       const u16* __restrict__ w_down, float* __restrict__ A, int T) {
    int t = blockIdx.x * blockDim.x + threadIdx.x;
    if (t >= T) return;
    int i = idx_in[t], o = idx_out[t];
    float a = bf2f(angle[t]);
    float c0 = cosf(bf2f(ang_freq[0]) * a), c1 = cosf(bf2f(ang_freq[1]) * a);
    float mv[4];
    for (int k = 0; k < 4; k++) mv[k] = bf2f(m[i * 4 + k]);
    for (int d = 0; d < 4; d++) {
        float md = 0.f;
        for (int k = 0; k < 4; k++) md += mv[k] * bf2f(w_down[k * 4 + d]);
        atomicAdd(&A[o * 8 + d], c0 * md);
        atomicAdd(&A[o * 8 + 4 + d], c1 * md);
    }
}

__global__ void k_fin(const u16* __restrict__ m, const u16* __restrict__ dist, const u16* __restrict__ centers,
                      const u16* __restrict__ w_rbf, const u16* __restrict__ w_sbf, const u16* __restrict__ w_up,
                      const float* __restrict__ A, u16* __restrict__ out, int E) {
    int e = blockIdx.x * blockDim.x + threadIdx.x;
    if (e >= E) return;
    float dv = bf2f(dist[e]);
    float rbf[8];
    for (int r = 0; r < 8; r++) { float x = dv - bf2f(centers[r]); rbf[r] = expf(-10.f * x * x); }
    float agg[4];
    for (int d = 0; d < 4; d++) {
        float s = 0.f;
        for (int l = 0; l < 2; l++) {
            float g = 0.f;
            for (int r = 0; r < 8; r++) g += rbf[r] * bf2f(w_sbf[(r * 2 + l) * 4 + d]);
            s += g * A[e * 8 + l * 4 + d];
        }
        agg[d] = s;
    }
    for (int d = 0; d < 4; d++) {
        float u = 0.f, rw = 0.f;
        for (int k = 0; k < 4; k++) u += agg[k] * bf2f(w_up[k * 4 + d]);
        for (int r = 0; r < 8; r++) rw += rbf[r] * bf2f(w_rbf[r * 4 + d]);
        out[e * 4 + d] = f2bf(bf2f(m[e * 4 + d]) + u * rw);
    }
}

extern "C" void kernel_launch(void* const* d_in, const int* in_sizes, int n_in,
                              void* d_out, int out_size, void* d_ws, size_t ws_size,
                              hipStream_t stream) {
    const u16* m = (const u16*)d_in[0];
    const int* idx_in = (const int*)d_in[1];
    const int* idx_out = (const int*)d_in[2];
    const u16* dist = (const u16*)d_in[3];
    const u16* angle = (const u16*)d_in[4];
    const u16* centers = (const u16*)d_in[5];
    const u16* ang_freq = (const u16*)d_in[6];
    const u16* w_rbf = (const u16*)d_in[7];
    const u16* w_sbf = (const u16*)d_in[8];
    const u16* w_down = (const u16*)d_in[9];
    const u16* w_up = (const u16*)d_in[10];
    int E = in_sizes[3], T = in_sizes[1];
    float* A = (float*)d_ws;
    k_zero<<<(E * 8 + 255) / 256, 256, 0, stream>>>(A, E * 8);
    k_trip<<<(T + 255) / 256, 256, 0, stream>>>(m, idx_in, idx_out, angle, ang_freq, w_down, A, T);
    k_fin<<<(E + 255) / 256, 256, 0, stream>>>(m, dist, centers, w_rbf, w_sbf, w_up, A, (u16*)d_out, E);
}
```

```cpp
#include <hip/hip_runtime.h>
#include <stdint.h>
#include <type_traits>

typedef unsigned int u32;
typedef unsigned short u16;
typedef unsigned long long u64;
typedef u32 u32x4 __attribute__((ext_vector_type(4)));
typedef u32 u32x3 __attribute__((ext_vector_type(3)));
typedef u32 u32x2 __attribute__((ext_vector_type(2)));
typedef u16 u16x2 __attribute__((ext_vector_type(2)));
typedef float f32x4 __attribute__((ext_vector_type(4)));
typedef __bf16 bf16x2 __attribute__((ext_vector_type(2)));

#define CFG_LD_AUX 0
#define CFG_ST_AUX 16
#define CFG_R1 266667
#define CFG_R2 533334

constexpr int E_ = 800000, T_ = 12800000;
constexpr u32 OOB = 0xfffffff0u;

constexpr int NBIN = 256;
constexpr int EPB = 3136;
constexpr int ACCN = 3144;
constexpr u32 DUMMY = EPB;
constexpr int NR = 3;
constexpr u32 R1 = CFG_R1, R2 = CFG_R2;
constexpr int NKEY = NBIN * NR;

constexpr int P1T = 1024, P1B = 256, CPB = 3, CH = 16668;
constexpr int IPB = T_ / P1B;
constexpr int NGRP = 4;
constexpr int MAINI = P1T * NGRP * 4;
constexpr int TAILMAX = CH - MAINI;
constexpr int NI = NGRP * 4 + 1;
constexpr int NCH = P1B * CPB;
static_assert(TAILMAX > 0 && TAILMAX <= P1T && IPB - (CPB - 1) * CH > MAINI && IPB * P1B == T_ && CH % 4 == 0 && IPB % 4 == 0, "chunks");
constexpr int NU = (CH / 2 + NKEY / 2 + 8 + 31) & ~31;
constexpr int ROWB = ((NBIN + 1) * CPB * 8 + 127) & ~127;
static_assert(NU < 65536 && NCH <= 1024, "table");

constexpr int WIN = 32768;
constexpr u32 MAXDEG = 512;

constexpr size_t WS_REC = 0;
constexpr size_t WS_REC_BYTES = (size_t)NCH * NU * 12;
constexpr size_t WS_TAB = WS_REC + WS_REC_BYTES;
constexpr size_t WS_TAB_BYTES = (size_t)P1B * ROWB;
constexpr size_t WS_WIDE = WS_TAB + WS_TAB_BYTES;
constexpr size_t WS_WIDE_BYTES = (size_t)NBIN * EPB * 64;
static_assert(WS_WIDE % 128 == 0 && WS_WIDE + WS_WIDE_BYTES <= ((size_t)256 << 20), "workspace");

__device__ __forceinline__ float bf2f(u32 x) { return __uint_as_float(x << 16); }
__device__ __forceinline__ u32 pack_bf16(float a, float b) { const bf16x2 v = {(__bf16)a, (__bf16)b}; return __builtin_bit_cast(u32, v); }
__device__ __forceinline__ __amdgpu_buffer_rsrc_t rsrc(const void* p, u32 bytes) {
    return __builtin_amdgcn_make_buffer_rsrc((void*)p, 0, bytes, 0x00020000);
}
__device__ __forceinline__ u32 wave_scan(u32 v) {
    v += (u32)__builtin_amdgcn_update_dpp(0, (int)v, 0x111, 0xf, 0xf, true);
    v += (u32)__builtin_amdgcn_update_dpp(0, (int)v, 0x112, 0xf, 0xf, true);
    v += (u32)__builtin_amdgcn_update_dpp(0, (int)v, 0x114, 0xf, 0xf, true);
    v += (u32)__builtin_amdgcn_update_dpp(0, (int)v, 0x118, 0xf, 0xf, true);
    v += (u32)__builtin_amdgcn_update_dpp(0, (int)v, 0x142, 0xa, 0xf, false);
    v += (u32)__builtin_amdgcn_update_dpp(0, (int)v, 0x143, 0xc, 0xf, false);
    return v;
}
__device__ __forceinline__ u32 wave_max(u32 v) {
    v = max(v, (u32)__builtin_amdgcn_update_dpp(0, (int)v, 0x111, 0xf, 0xf, true));
    v = max(v, (u32)__builtin_amdgcn_update_dpp(0, (int)v, 0x112, 0xf, 0xf, true));
    v = max(v, (u32)__builtin_amdgcn_update_dpp(0, (int)v, 0x114, 0xf, 0xf, true));
    v = max(v, (u32)__builtin_amdgcn_update_dpp(0, (int)v, 0x118, 0xf, 0xf, true));
    v = max(v, (u32)__builtin_amdgcn_update_dpp(0, (int)v, 0x142, 0xa, 0xf, false));
    v = max(v, (u32)__builtin_amdgcn_update_dpp(0, (int)v, 0x143, 0xc, 0xf, false));
    return v;
}
__device__ __forceinline__ u32 pk_max(u32 a, u32 b) {
    return __builtin_bit_cast(u32, __builtin_elementwise_max(__builtin_bit_cast(u16x2, a), __builtin_bit_cast(u16x2, b)));
}

#define STAMP(k, w)

__global__ __launch_bounds__(P1T, 4) void p1_kernel(const u32* __restrict__ idx_in, const u32* __restrict__ idx_out,
                                                    const u16* __restrict__ angle, const u32* __restrict__ mtab,
                                                    u32* __restrict__ rec, u32* __restrict__ tab) {
    __shared__ __attribute__((aligned(16))) u64 U64[NU];
    __shared__ __attribute__((aligned(16))) u32 U32[NU];
    __shared__ __attribute__((aligned(16))) u32 cnt[NKEY];
    __shared__ __attribute__((aligned(16))) u32 cur[NKEY];
    __shared__ __attribute__((aligned(16))) u32 tabrow[ROWB / 4];
    __shared__ u32 misc[2];
    STAMP(0, 0);
    int t = threadIdx.x;
    const int b0 = blockIdx.x * IPB;
    const __amdgpu_buffer_rsrc_t r_in = rsrc(idx_in + b0, IPB * 4), r_out = rsrc(idx_out + b0, IPB * 4), r_ang = rsrc(angle + b0, IPB * 2);

    u32x4 I[NGRP], O[NGRP];
    u32x2 A[NGRP];
    u32 It, Ot, At;
    constexpr int G0 = (NGRP + 2) / 3, G1 = (2 * NGRP + 2) / 3;
    auto load_part = [&](int ci, int part) {
        const u32 base = ci < CPB ? ci * CH : 0x10000000u;
        const int k0 = part == 0 ? 0 : part == 1 ? G0 : G1, k1 = part == 0 ? G0 : part == 1 ? G1 : NGRP;
#pragma unroll
        for (int k = 0; k < NGRP; k++) {
            if (k >= k0 && k < k1) {
                const u32 e = base + (t + P1T * k) * 4;
                I[k] = __builtin_amdgcn_raw_buffer_load_b128(r_in, e * 4, 0, CFG_LD_AUX);
                O[k] = __builtin_amdgcn_raw_buffer_load_b128(r_out, e * 4, 0, CFG_LD_AUX);
                A[k] = __builtin_amdgcn_raw_buffer_load_b64(r_ang, e * 2, 0, CFG_LD_AUX);
            }
        }
        if (part == 2) {
            const u32 e = t < TAILMAX ? base + MAINI + t : 0x10000000u;
            It = __builtin_amdgcn_raw_buffer_load_b32(r_in, e * 4, 0, CFG_LD_AUX);
            Ot = __builtin_amdgcn_raw_buffer_load_b32(r_out, e * 4, 0, CFG_LD_AUX);
            At = __builtin_amdgcn_raw_buffer_load_b16(r_ang, e * 2, 0, CFG_LD_AUX);
        }
        __builtin_amdgcn_sched_barrier(0);
    };
    u32 X[NI], Y[NI];
    auto keys = [&]() {
        auto key = [&](int q, u32 in, u32 o, u32 ang) {
            X[q] = (((o >> 8) & ~15u) | (o & 15)) | (in << 12);
            const u32 r = in < R1 ? 0u : in < R2 ? 1u : 2u;
            Y[q] = ang | ((r * NBIN + ((o >> 4) & (NBIN - 1))) << 16);
            asm volatile("" : "+v"(X[q]), "+v"(Y[q]));
        };
        u32 am = At & 0x7fffu;
#pragma unroll
        for (int k = 0; k < NGRP; k++) {
            am = pk_max(am, pk_max(A[k].x & 0x7fff7fffu, A[k].y & 0x7fff7fffu));
#pragma unroll
            for (int j = 0; j < 4; j++) key(k * 4 + j, I[k][j], O[k][j], (j & 1) ? (A[k][j >> 1] >> 16) : (A[k][j >> 1] & 0xffff));
        }
        key(NI - 1, It, Ot, At);
        if ((am & 0x7f80u) == 0x7f80u || (am & 0x7f800000u) == 0x7f800000u) atomicMax(&misc[0], 0x8000u);
        __builtin_amdgcn_sched_barrier(0);
    };
    auto count = [&](bool tail) {
#pragma unroll
        for (int q = 0; q < NI - 1; q++) atomicAdd(&cnt[Y[q] >> 16], 1u);
        if (tail) atomicAdd(&cnt[Y[NI - 1] >> 16], 1u);
    };
    auto ntail = [&](int ci) { return (ci == CPB - 1 ? IPB - (CPB - 1) * CH : CH) - MAINI; };

    load_part(0, 0); load_part(0, 1); load_part(0, 2);
    {
        constexpr int MW = (E_ * 8 / 16 + P1B - 1) / P1B;
        constexpr int NJ = (MW + P1T - 1) / P1T;
        const __amdgpu_buffer_rsrc_t r_m = rsrc(mtab, E_ * 8);
        u32 mx = 0;
#pragma unroll
        for (int j = 0; j < NJ; j++) {
            const int w = t + P1T * j;
            const u32x4 mv = __builtin_amdgcn_raw_buffer_load_b128(r_m, w < MW ? (blockIdx.x * MW + w) * 16 : OOB, 0, 0);
            mx = pk_max(mx, pk_max(pk_max(mv.x & 0x7fff7fffu, mv.y & 0x7fff7fffu), pk_max(mv.z & 0x7fff7fffu, mv.w & 0x7fff7fffu)));
        }
        if (t < 2) misc[t] = 0;
        for (int i = t; i < NKEY; i += P1T) cnt[i] = 0;
        for (int i = t; i < ROWB / 4; i += P1T) tabrow[i] = 0;
        __syncthreads();
        mx = wave_max(max(mx >> 16, mx & 0xffff));
        if ((t & 63) == 63) atomicMax(&misc[0], mx);
    }
    keys();
    load_part(1, 0);
    count(t < ntail(0));
    load_part(1, 1);
#pragma unroll 1
    for (int ci = 0; ci < CPB; ci++) {
        asm volatile("" : "+v"(t));
        const int c = blockIdx.x * CPB + ci;
        const bool tail = t < ntail(ci);
        __syncthreads();
        if (t < 64) {
            u32 sbase = 0;
            u32 pk[NR][2], end[NR];
#pragma unroll
            for (int s = 0; s < NR; s++) {
                const u32x4 h = *(const u32x4*)&cnt[s * NBIN + t * 4];
                *(u32x4*)&cnt[s * NBIN + t * 4] = (u32x4){0, 0, 0, 0};
                const u32x4 un = (h + 1u) >> 1;
                const u32 sum = un.x + un.y + un.z + un.w;
                const u32 inc = wave_scan(sum);
                const u32 tot = __builtin_amdgcn_readlane(inc, 63);
                const u32 p0 = sbase + inc - sum, p1 = p0 + un.x, p2 = p1 + un.y, p3 = p2 + un.z;
                const u32x4 p = {p0, p1, p2, p3};
#pragma unroll
                for (int j = 0; j < 4; j++)
                    if (h[j] & 1) {
                        const u32 q = 2 * p[j] + h[j];
                        ((u32*)U64)[q] = DUMMY | 0xfffff000u;
                        ((u16*)U32)[q] = 0;
                    }
                *(u32x4*)&cur[s * NBIN + t * 4] = p + p;
                pk[s][0] = p0 | (p1 << 16);
                pk[s][1] = p2 | (p3 << 16);
                sbase += tot;
                end[s] = sbase;
            }
            u32* row = tabrow + ci * 2;
            const u32 mhi = misc[0] << 16;
#pragma unroll
            for (int j = 0; j < 4; j++) {
                const u32 o0 = (pk[0][j >> 1] >> (16 * (j & 1))) & 0xffff, o1 = (pk[1][j >> 1] >> (16 * (j & 1))) & 0xffff;
                const u32 o2 = (pk[2][j >> 1] >> (16 * (j & 1))) & 0xffff;
                *(u32x2*)&row[(t * 4 + j) * (CPB * 2)] = (u32x2){o0 | (o1 << 16), o2 | mhi};
            }
            if (t == 0) {
                *(u32x2*)&row[NBIN * (CPB * 2)] = (u32x2){end[0] | (end[1] << 16), end[2] | mhi};
                misc[1] = sbase;
            }
        }
        __syncthreads();
        load_part(ci + 1, 2);
        auto scatter = [&](int q0, int q1) {
            u32 pos[8];
#pragma unroll
            for (int q = q0; q < q1; q++) pos[q - q0] = atomicAdd(&cur[Y[q] >> 16], 1u);
#pragma unroll
            for (int q = q0; q < q1; q++) {
                ((u32*)U64)[pos[q - q0]] = X[q];
                ((u16*)U32)[pos[q - q0]] = (u16)Y[q];
            }
        };
        scatter(0, 8);
        __builtin_amdgcn_sched_barrier(0);
        scatter(8, 16);
        if (tail) scatter(16, 17);
        __syncthreads();
        const u32 total = __builtin_amdgcn_readfirstlane(misc[1]);
        const bool more = ci + 1 < CPB;
        if (more) keys();
        load_part(ci + 2, 0);
        const __amdgpu_buffer_rsrc_t r_rec = rsrc(rec + (size_t)c * NU * 3, (total * 12 + 127) & ~127u);
#pragma unroll
        for (int j = 0; j < (NU + P1T - 1) / P1T; j++) {
            const u32 u = min((u32)(t + P1T * j), (u32)(NU - 1));
            const u64 v = U64[u];
            const u32 w = U32[u];
            __builtin_amdgcn_raw_buffer_store_b96((u32x3){(u32)v, (u32)(v >> 32), w}, r_rec, (t + P1T * j) * 12, 0, CFG_ST_AUX);
        }
        __builtin_amdgcn_sched_barrier(0);
        load_part(ci + 2, 1);
        if (more) count(t < ntail(ci + 1));
    }
    for (int i = t; i < ROWB / 16; i += P1T) ((u32x4*)(tab + (size_t)blockIdx.x * (ROWB / 4)))[i] = ((const u32x4*)tabrow)[i];
    STAMP(0, 1);
}

struct __attribute__((aligned(16))) Desc { u32 lo, hi, base, pad; };
struct P2Shared {
    u64 acc[4][ACCN];
    Desc desc[WIN / 64];
    u32 deg[ACCN];
    u32 delta[NR * NCH];
    u64 wsum[NR][16];
    u32 wmax[16];
    u32 misc[4];
    __attribute__((aligned(16))) float xg[3][64][4];
    __attribute__((aligned(16))) float w[144];
};

__device__ __forceinline__ void accumulate(P2Shared& sh, u32 row, u32 angbits, u32x2 m, float fr0, float fr1, float S) {
    const float a = __uint_as_float(angbits);
    const float c0 = __builtin_amdgcn_cosf(__builtin_amdgcn_fractf(a * fr0)) * S;
    const float c1 = __builtin_amdgcn_cosf(__builtin_amdgcn_fractf(a * fr1)) * S;
    const float m0 = __uint_as_float(m.x << 16), m1 = __uint_as_float(m.x & 0xffff0000u);
    const float m2 = __uint_as_float(m.y << 16), m3 = __uint_as_float(m.y & 0xffff0000u);
    const float K = 12582912.0f;
    const u32 t0 = __float_as_uint(__builtin_fmaf(c0, m0, K)), t1 = __float_as_uint(__builtin_fmaf(c0, m1, K));
    const u32 t2 = __float_as_uint(__builtin_fmaf(c0, m2, K)), t3 = __float_as_uint(__builtin_fmaf(c0, m3, K));
    const u32 t4 = __float_as_uint(__builtin_fmaf(c1, m0, K)), t5 = __float_as_uint(__builtin_fmaf(c1, m1, K));
    const u32 t6 = __float_as_uint(__builtin_fmaf(c1, m2, K)), t7 = __float_as_uint(__builtin_fmaf(c1, m3, K));
    atomicAdd(&sh.acc[0][row], ((u64)t1 << 32) | t0);
    atomicAdd(&sh.acc[1][row], ((u64)t3 << 32) | t2);
    atomicAdd(&sh.acc[2][row], ((u64)t5 << 32) | t4);
    atomicAdd(&sh.acc[3][row], ((u64)t7 << 32) | t6);
    atomicAdd(&sh.deg[row], 1u);
}
__device__ __forceinline__ void accumulate_wide(double* __restrict__ wide, u32 row, u32 angbits, u32x2 mm, float fr0, float fr1) {
    if (row >= (u32)EPB) return;
    const float a = __uint_as_float(angbits);
    const float c0 = __builtin_amdgcn_cosf(__builtin_amdgcn_fractf(a * fr0));
    const float c1 = __builtin_amdgcn_cosf(__builtin_amdgcn_fractf(a * fr1));
    const float m[4] = {__uint_as_float(mm.x << 16), __uint_as_float(mm.x & 0xffff0000u), __uint_as_float(mm.y << 16), __uint_as_float(mm.y & 0xffff0000u)};
#pragma unroll
    for (int k = 0; k < 4; k++) {
        atomicAdd(&wide[row * 8 + k], (double)(c0 * m[k]));
        atomicAdd(&wide[row * 8 + 4 + k], (double)(c1 * m[k]));
    }
}

__global__ __launch_bounds__(1024, 4) void p2_kernel(const u32* __restrict__ rec, const u32* __restrict__ tab,
                                                     const u32* __restrict__ mtab, const u16* __restrict__ dist,
                                                     const u16* __restrict__ centers, const u16* __restrict__ ang_freq,
                                                     const u16* __restrict__ w_rbf, const u16* __restrict__ w_sbf,
                                                     const u16* __restrict__ w_down, const u16* __restrict__ w_up,
                                                     double* __restrict__ wide_all, u32* __restrict__ out) {
    __shared__ __attribute__((aligned(16))) P2Shared sh;
    STAMP(1, 0);
    int t = threadIdx.x;
    const int lane = t & 63;
    const int wave = __builtin_amdgcn_readfirstlane(t >> 6);
    const int bin = (blockIdx.x & 7) * (NBIN / 8) + (blockIdx.x >> 3);
    const __amdgpu_buffer_rsrc_t r_rec = rsrc(rec, (u32)WS_REC_BYTES), r_tab = rsrc(tab, (u32)WS_TAB_BYTES);
    const __amdgpu_buffer_rsrc_t r_m = rsrc(mtab, E_ * 8), r_dist = rsrc(dist, E_ * 2), r_res = rsrc(out, E_ * 8);
    double* wide = wide_all + (size_t)bin * EPB * 8;

    u32x2 e0, e1;
    auto load_table = [&]() {
        const u32 b = t / CPB, ci = t - b * CPB;
        const u32 off = t < NCH ? b * ROWB + (bin * CPB + ci) * 8 : OOB - CPB * 8;
        e0 = __builtin_amdgcn_raw_buffer_load_b64(r_tab, off, 0, 0);
        e1 = __builtin_amdgcn_raw_buffer_load_b64(r_tab, off + CPB * 8, 0, 0);
    };
    load_table();
    auto edge = [&](u32 el) -> u32 { return ((el >> 4) << 12) | (bin << 4) | (el & 15); };
    u32 dv4[4];
#pragma unroll
    for (int i = 0; i < 4; i++) dv4[i] = __builtin_amdgcn_raw_buffer_load_b16(r_dist, (t + 1024 * i < EPB) ? edge(t + 1024 * i) * 2 : OOB, 0, 0);
    auto wload = [&](const u16* p, int first, int n) -> u32 { return __builtin_amdgcn_raw_buffer_load_b16(rsrc(p, n * 2), (u32)(t - first) * 2, 0, 0); };
    const u32 wv0 = wload(w_sbf, 0, 64), wv1 = wload(w_rbf, 64, 32), wv2 = wload(w_down, 96, 16), wv3 = wload(w_up, 112, 16), wv4 = wload(centers, 128, 8);
    const u32 fbits = *(const u32*)ang_freq;
    const float fr0 = __uint_as_float(fbits << 16) * 0.15915494309189535f, fr1 = __uint_as_float(fbits & 0xffff0000u) * 0.15915494309189535f;
    __builtin_amdgcn_sched_barrier(0);
    for (int i = t; i < 4 * ACCN / 2; i += 1024) ((u32x4*)sh.acc)[i] = (u32x4){0, 0, 0, 0};
    for (int i = t; i < ACCN; i += 1024) sh.deg[i] = 0;
    if (t < WIN / 64) *(u32x4*)&sh.desc[t] = (u32x4){0, 0, 0, 0};
    if (t < 3) sh.misc[t] = t == 2 ? 32 : 0;
    __builtin_amdgcn_sched_barrier(0);
    if (t < 136) sh.w[t] = bf2f(wv0 | wv1 | wv2 | wv3 | wv4);
    {
        const u32 mx = wave_max(e0.y >> 16);
        if (lane == 63) sh.wmax[wave] = mx;
    }

    bool slow = false;
    float S = 1.0f, invS = 1.0f;
    u32 mbits = 0;
    u32 win = 0;
#pragma unroll 1
    for (;;) {
        asm volatile("" : "+v"(t));
        u32 len[NR], st[NR], inc[NR], ninc[NR];
        st[0] = e0.x & 0xffff; st[1] = e0.x >> 16; st[2] = e0.y & 0xffff;
        len[0] = (e1.x & 0xffff) - st[0]; len[1] = (e1.x >> 16) - st[1]; len[2] = (e1.y & 0xffff) - st[2];
#pragma unroll
        for (int s = 0; s < NR; s++) {
            inc[s] = wave_scan(len[s]);
            const u64 ne = __ballot(len[s] != 0);
            ninc[s] = __builtin_amdgcn_mbcnt_hi((u32)(ne >> 32), __builtin_amdgcn_mbcnt_lo((u32)ne, 0));
            const u32 wt = __builtin_amdgcn_readlane(inc[s], 63);
            if (lane == 0) sh.wsum[s][wave] = (u64)wt | ((u64)__popcll(ne) << 32);
        }
        u32 warm = 0;
        if (win == 0 && !slow) {
            constexpr u32 lines = (R1 * 8 / 128 + 31) / 32;
            warm = __builtin_amdgcn_raw_buffer_load_b32(r_m, (u32)t < lines ? ((blockIdx.x >> 3) * lines + t) * 128 : OOB, 0, 0);
        }
        __syncthreads();
        if (win == 0 && !slow) {
            mbits = __builtin_amdgcn_readlane(wave_max(lane < 16 ? sh.wmax[lane] : 0), 63);
            const int ex = max((int)((mbits >> 7) & 0xff) - 127 + 1, -100);
            S = __uint_as_float((u32)(22 - ex + 127) << 23);
            invS = __uint_as_float((u32)(127 - 22 + ex) << 23);
            if ((fbits & 0x7f80u) == 0x7f80u || (fbits & 0x7f800000u) == 0x7f800000u) mbits = 0x8000u;
        }
        const u32 W0 = win * WIN;
        u32 L = 0, nseg = 0;
#pragma unroll
        for (int s = 0; s < NR; s++) {
            const u64 x = lane < 16 ? sh.wsum[s][lane] : 0;
            const u32 sl = wave_scan((u32)x), sn = wave_scan((u32)(x >> 32));
            const u32 P = L + __builtin_amdgcn_readlane(sl - (u32)x, wave) + inc[s] - len[s];
            const u32 ord = nseg + __builtin_amdgcn_readlane(sn - (u32)(x >> 32), wave) + ninc[s];
            L += __builtin_amdgcn_readlane(sl, 63);
            nseg += __builtin_amdgcn_readlane(sn, 63);
            if (len[s]) {
                sh.delta[ord] = (u32)t * NU + st[s] - P;
                const u32 p = P - W0;
                if (p < WIN) atomicOr((p & 32) ? &sh.desc[p >> 6].hi : &sh.desc[p >> 6].lo, 1u << (p & 31));
                const u32 end = P + len[s];
                if (end >= W0 && P < W0 + WIN) {
                    u32 w = P < W0 ? 0 : (p >> 6) + 1;
                    const u32 w1 = min((end - W0) >> 6, (u32)(WIN / 64 - 1));
                    for (; w <= w1; w++) sh.desc[w].base = ord + 1;
                }
            }
        }
        const u32 nwin = (L + WIN - 1) / WIN;
        __syncthreads();

        const u32 rem = L > W0 ? min(L - W0, (u32)WIN) : 0;
        const u32 nit = (rem + 63) >> 6;

        auto fetch = [&](u32 k) -> u32x3 {
            const u32x4 d = *(const u32x4*)&sh.desc[min(k, (u32)(WIN / 64 - 1))];
            const u32 lo = __builtin_amdgcn_readfirstlane(d.x), hi = __builtin_amdgcn_readfirstlane(d.y), b = __builtin_amdgcn_readfirstlane(d.z);
            const u64 w1 = (((u64)hi << 32) | lo) >> 1;
            const u32 s = b + (lo & 1) - 1 + __builtin_amdgcn_mbcnt_hi((u32)(w1 >> 32), __builtin_amdgcn_mbcnt_lo((u32)w1, 0));
            const u32 i = k * 64 + lane;
            const bool ok = i < rem;
            const u32 dd = sh.delta[ok ? s : 0];
            return __builtin_amdgcn_raw_buffer_load_b96(r_rec, ok ? (dd + W0 + i) * 12 : OOB, 0, 0);
        };
        auto gather = [&](const u32x3& r, u32 k, u32x2& g0, u32x2& g1) {
            const bool ok = k * 64 + lane < rem;
            g0 = __builtin_amdgcn_raw_buffer_load_b64(r_m, ok ? (r.x >> 12) * 8 : OOB, 0, 0);
            g1 = __builtin_amdgcn_raw_buffer_load_b64(r_m, ok ? (r.y >> 12) * 8 : OOB, 0, 0);
        };
        auto ticket = [&]() -> u32 { u32 v = 0; if (lane == 0) v = atomicAdd(&sh.misc[2], 1u); return v; };

        float G[3][8], rw[3][4];
        u32x2 mm[3];
        auto edge_terms = [&](auto NEc, auto I0c, auto& Go, auto& rwo) {
            constexpr int NE = decltype(NEc)::value, I0 = decltype(I0c)::value;
#pragma unroll
            for (int i = 0; i < NE; i++) {
#pragma unroll
                for (int q = 0; q < 8; q++) Go[i][q] = 0;
#pragma unroll
                for (int q = 0; q < 4; q++) rwo[i][q] = 0;
            }
#pragma unroll 1
            for (int r = 0; r < 8; r++) {
                const f32x4 w0 = *(const f32x4*)&sh.w[r * 8], w1 = *(const f32x4*)&sh.w[r * 8 + 4], w2 = *(const f32x4*)&sh.w[64 + r * 4];
                const float cr = sh.w[128 + r];
#pragma unroll
                for (int i = 0; i < NE; i++) {
                    const float x = bf2f(dv4[I0 + i]) - cr;
                    const float rb = __builtin_amdgcn_exp2f(-14.426950408889634f * x * x);
#pragma unroll
                    for (int q = 0; q < 4; q++) { Go[i][q] += rb * w0[q]; Go[i][4 + q] += rb * w1[q]; rwo[i][q] += rb * w2[q]; }
                }
            }
        };
        auto all_edge_terms = [&]() {
            if (wave == 0) {
                float Gx[1][8], rwx[1][4];
                edge_terms(std::integral_constant<int, 1>{}, std::integral_constant<int, 3>{}, Gx, rwx);
                *(f32x4*)&sh.xg[0][lane][0] = (f32x4){Gx[0][0], Gx[0][1], Gx[0][2], Gx[0][3]};
                *(f32x4*)&sh.xg[1][lane][0] = (f32x4){Gx[0][4], Gx[0][5], Gx[0][6], Gx[0][7]};
                *(f32x4*)&sh.xg[2][lane][0] = (f32x4){rwx[0][0], rwx[0][1], rwx[0][2], rwx[0][3]};
            }
            edge_terms(std::integral_constant<int, 3>{}, std::integral_constant<int, 0>{}, G, rw);
        };
        auto finish = [&](auto NEc, auto I0c, bool wide_sums) -> bool {
            constexpr int NE = decltype(NEc)::value, I0 = decltype(I0c)::value;
            bool bad = false;
#pragma unroll
            for (int i = 0; i < NE; i++) {
                asm volatile("" ::: "memory");
                const int el = t + 1024 * (I0 + i);
                float Aa[8];
                if (wide_sums) {
#pragma unroll
                    for (int q = 0; q < 8; q++) Aa[q] = (float)__hip_atomic_load(&wide[el * 8 + q], __ATOMIC_RELAXED, __HIP_MEMORY_SCOPE_AGENT);
                } else {
                    const u32 dg = sh.deg[el];
                    bad |= dg > MAXDEG;
                    const u64 kk = (u64)dg * 0x4B4000004B400000ull;
#pragma unroll
                    for (int q = 0; q < 4; q++) {
                        const u64 v = sh.acc[q][el] - kk;
                        const int lo = (int)(u32)v;
                        const int hi = (int)((u32)(v >> 32) - (u32)(lo >> 31));
                        Aa[2 * q] = (float)lo * invS;
                        Aa[2 * q + 1] = (float)hi * invS;
                    }
                }
                f32x4 b0 = {0, 0, 0, 0}, b1 = {0, 0, 0, 0}, u = {0, 0, 0, 0};
#pragma unroll
                for (int k = 0; k < 4; k++) { const f32x4 wd = *(const f32x4*)&sh.w[96 + k * 4]; b0 += Aa[k] * wd; b1 += Aa[4 + k] * wd; }
#pragma unroll
                for (int k = 0; k < 4; k++) u += (G[i][k] * b0[k] + G[i][4 + k] * b1[k]) * *(const f32x4*)&sh.w[112 + k * 4];
                const float o0 = __uint_as_float(mm[i].x << 16) + u[0] * rw[i][0], o1 = __uint_as_float(mm[i].x & 0xffff0000u) + u[1] * rw[i][1];
                const float o2 = __uint_as_float(mm[i].y << 16) + u[2] * rw[i][2], o3 = __uint_as_float(mm[i].y & 0xffff0000u) + u[3] * rw[i][3];
                __builtin_amdgcn_raw_buffer_store_b64((u32x2){pack_bf16(o0, o1), pack_bf16(o2, o3)}, r_res, edge(el) * 8, 0, 0);
            }
            return bad;
        };

        auto mainloop = [&](auto SLOWc) {
            constexpr bool SLOW = decltype(SLOWc)::value;
            u32x3 cur, nxt;
            u32 kc = wave, kn = wave + 16, tk = 0;
            const bool any = kc < nit;
            if (any) {
                tk = ticket();
                cur = fetch(kc);
                nxt = fetch(kn);
            }
            __builtin_amdgcn_sched_barrier(0);
            all_edge_terms();
            asm volatile("" :: "v"(warm));
            __builtin_amdgcn_sched_barrier(0);
            if (any) {
                u32x2 c0, c1;
                gather(cur, kc, c0, c1);
                while (kc < nit) {
                    u32x2 n0, n1;
                    gather(nxt, kn, n0, n1);
                    const u32 knn = __builtin_amdgcn_readfirstlane(tk);
                    tk = ticket();
                    const u32x3 nn = fetch(knn);
                    __builtin_amdgcn_sched_barrier(0);
                    if (kc * 64 + lane < rem) {
                        if (SLOW) {
                            accumulate_wide(wide, cur.x & 0xfff, cur.z << 16, c0, fr0, fr1);
                            accumulate_wide(wide, cur.y & 0xfff, cur.z & 0xffff0000u, c1, fr0, fr1);
                        } else {
                            accumulate(sh, cur.x & 0xfff, cur.z << 16, c0, fr0, fr1, S);
                            accumulate(sh, cur.y & 0xfff, cur.z & 0xffff0000u, c1, fr0, fr1, S);
                        }
                    }
                    __builtin_amdgcn_sched_barrier(0);
                    cur = nxt; nxt = nn; c0 = n0; c1 = n1; kc = kn; kn = knn;
                }
            }
        };
        if (slow) { mainloop(std::true_type{}); asm volatile("s_waitcnt vmcnt(0)" ::: "memory"); }
        else mainloop(std::false_type{});

        win++;
        if (win >= nwin) {
#pragma unroll
            for (int i = 0; i < 3; i++) mm[i] = __builtin_amdgcn_raw_buffer_load_b64(r_m, edge(t + 1024 * i) * 8, 0, 0);
            const u32x2 mmx = __builtin_amdgcn_raw_buffer_load_b64(r_m, t < EPB - 3072 ? edge(t + 3072) * 8 : OOB, 0, 0);
            __syncthreads();
            if (slow) __threadfence();
            bool bad = finish(std::integral_constant<int, 3>{}, std::integral_constant<int, 0>{}, slow);
            if (wave == 0) {
                const f32x4 g0 = *(const f32x4*)&sh.xg[0][lane][0], g1 = *(const f32x4*)&sh.xg[1][lane][0], g2 = *(const f32x4*)&sh.xg[2][lane][0];
#pragma unroll
                for (int q = 0; q < 4; q++) { G[0][q] = g0[q]; G[0][4 + q] = g1[q]; rw[0][q] = g2[q]; }
                mm[0] = mmx;
                bad |= finish(std::integral_constant<int, 1>{}, std::integral_constant<int, 3>{}, slow);
            }
            if (slow) break;
            if (bad || mbits >= 0x7f80) sh.misc[1] = 1;
            __syncthreads();
            if (!sh.misc[1]) break;
            slow = true;
            win = 0;
            for (int i = t; i < EPB * 8; i += 1024) __hip_atomic_store(&wide[i], 0.0, __ATOMIC_RELAXED, __HIP_MEMORY_SCOPE_AGENT);
            __threadfence();
        } else {
            __syncthreads();
        }
        load_table();
        if (t < WIN / 64) *(u32x4*)&sh.desc[t] = (u32x4){0, 0, 0, 0};
        if (t == 0) sh.misc[2] = 32;
        __syncthreads();
    }
    STAMP(1, 1);
}

extern "C" void kernel_launch(void* const* d_in, const int* in_sizes, int n_in, void* d_out, int out_size, void* d_ws, size_t ws_size,
                              hipStream_t stream) {
    const u32* m = (const u32*)d_in[0];
    const u32* idx_in = (const u32*)d_in[1];
    const u32* idx_out = (const u32*)d_in[2];
    const u16* dist = (const u16*)d_in[3];
    const u16* angle = (const u16*)d_in[4];
    const u16* centers = (const u16*)d_in[5];
    const u16* ang_freq = (const u16*)d_in[6];
    const u16* w_rbf = (const u16*)d_in[7];
    const u16* w_sbf = (const u16*)d_in[8];
    const u16* w_down = (const u16*)d_in[9];
    const u16* w_up = (const u16*)d_in[10];
    char* ws = (char*)d_ws;
    u32* rec = (u32*)(ws + WS_REC);
    u32* tab = (u32*)(ws + WS_TAB);
    p1_kernel<<<P1B, P1T, 0, stream>>>(idx_in, idx_out, angle, m, rec, tab);
    p2_kernel<<<NBIN, 1024, 0, stream>>>(rec, tab, m, dist, centers, ang_freq, w_rbf, w_sbf, w_down, w_up, (double*)(ws + WS_WIDE), (u32*)d_out);
}
```

```cpp
#include <hip/hip_runtime.h>
#include <stdint.h>
#include <type_traits>

typedef unsigned int u32;
typedef unsigned short u16;
typedef unsigned long long u64;
typedef u32 u32x4 __attribute__((ext_vector_type(4)));
typedef u32 u32x3 __attribute__((ext_vector_type(3)));
typedef u32 u32x2 __attribute__((ext_vector_type(2)));
typedef u16 u16x2 __attribute__((ext_vector_type(2)));
typedef float f32x4 __attribute__((ext_vector_type(4)));
typedef __bf16 bf16x2 __attribute__((ext_vector_type(2)));

#define CFG_LD_AUX 0
#define CFG_ST_AUX 16
#define CFG_R1 266667
#define CFG_R2 533334

constexpr int E_ = 800000, T_ = 12800000;
constexpr u32 OOB = 0xfffffff0u;

constexpr int NBIN = 256;
constexpr int EPB = 3136;
constexpr int ACCN = 3144;
constexpr u32 DUMMY = EPB;
constexpr int NR = 3;
constexpr u32 R1 = CFG_R1, R2 = CFG_R2;
constexpr int NKEY = NBIN * NR;

constexpr int P1T = 1024, P1B = 256, CPB = 3, CH = 16668;
constexpr int IPB = T_ / P1B;
constexpr int NGRP = 4;
constexpr int MAINI = P1T * NGRP * 4;
constexpr int TAILMAX = CH - MAINI;
constexpr int NI = NGRP * 4 + 1;
constexpr int NCH = P1B * CPB;
static_assert(TAILMAX > 0 && TAILMAX <= P1T && IPB - (CPB - 1) * CH > MAINI && IPB * P1B == T_ && CH % 4 == 0 && IPB % 4 == 0, "chunks");
constexpr int NU = (CH / 2 + NKEY / 2 + 8 + 31) & ~31;
constexpr int ROWB = ((NBIN + 1) * CPB * 8 + 127) & ~127;
static_assert(NU < 65536 && NCH <= 1024, "table");

constexpr int WIN = 32768;
constexpr u32 MAXDEG = 512;

constexpr size_t WS_REC = 0;
constexpr size_t WS_REC_BYTES = (size_t)NCH * NU * 12;
constexpr size_t WS_TAB = WS_REC + WS_REC_BYTES;
constexpr size_t WS_TAB_BYTES = (size_t)P1B * ROWB;
constexpr size_t WS_WIDE = WS_TAB + WS_TAB_BYTES;
constexpr size_t WS_WIDE_BYTES = (size_t)NBIN * EPB * 64;
static_assert(WS_WIDE % 128 == 0 && WS_WIDE + WS_WIDE_BYTES <= ((size_t)256 << 20), "workspace");

__device__ __forceinline__ float bf2f(u32 x) { return __uint_as_float(x << 16); }
__device__ __forceinline__ u32 pack_bf16(float a, float b) { const bf16x2 v = {(__bf16)a, (__bf16)b}; return __builtin_bit_cast(u32, v); }
__device__ __forceinline__ __amdgpu_buffer_rsrc_t rsrc(const void* p, u32 bytes) {
    return __builtin_amdgcn_make_buffer_rsrc((void*)p, 0, bytes, 0x00020000);
}
__device__ __forceinline__ u32 wave_scan(u32 v) {
    v += (u32)__builtin_amdgcn_update_dpp(0, (int)v, 0x111, 0xf, 0xf, true);
    v += (u32)__builtin_amdgcn_update_dpp(0, (int)v, 0x112, 0xf, 0xf, true);
    v += (u32)__builtin_amdgcn_update_dpp(0, (int)v, 0x114, 0xf, 0xf, true);
    v += (u32)__builtin_amdgcn_update_dpp(0, (int)v, 0x118, 0xf, 0xf, true);
    v += (u32)__builtin_amdgcn_update_dpp(0, (int)v, 0x142, 0xa, 0xf, false);
    v += (u32)__builtin_amdgcn_update_dpp(0, (int)v, 0x143, 0xc, 0xf, false);
    return v;
}
__device__ __forceinline__ u32 wave_max(u32 v) {
    v = max(v, (u32)__builtin_amdgcn_update_dpp(0, (int)v, 0x111, 0xf, 0xf, true));
    v = max(v, (u32)__builtin_amdgcn_update_dpp(0, (int)v, 0x112, 0xf, 0xf, true));
    v = max(v, (u32)__builtin_amdgcn_update_dpp(0, (int)v, 0x114, 0xf, 0xf, true));
    v = max(v, (u32)__builtin_amdgcn_update_dpp(0, (int)v, 0x118, 0xf, 0xf, true));
    v = max(v, (u32)__builtin_amdgcn_update_dpp(0, (int)v, 0x142, 0xa, 0xf, false));
    v = max(v, (u32)__builtin_amdgcn_update_dpp(0, (int)v, 0x143, 0xc, 0xf, false));
    return v;
}
__device__ __forceinline__ u32 pk_max(u32 a, u32 b) {
    return __builtin_bit_cast(u32, __builtin_elementwise_max(__builtin_bit_cast(u16x2, a), __builtin_bit_cast(u16x2, b)));
}

__global__ __launch_bounds__(P1T, 4) void p1_kernel(const u32* __restrict__ idx_in, const u32* __restrict__ idx_out,
                                                    const u16* __restrict__ angle, const u32* __restrict__ mtab,
                                                    u32* __restrict__ rec, u32* __restrict__ tab) {
    __shared__ __attribute__((aligned(16))) u64 U64[NU];
    __shared__ __attribute__((aligned(16))) u32 U32[NU];
    __shared__ __attribute__((aligned(16))) u32 cnt[NKEY];
    __shared__ __attribute__((aligned(16))) u32 cur[NKEY];
    __shared__ __attribute__((aligned(16))) u32 tabrow[ROWB / 4];
    __shared__ u32 misc[2];
    int t = threadIdx.x;
    const int b0 = blockIdx.x * IPB;
    const __amdgpu_buffer_rsrc_t r_in = rsrc(idx_in + b0, IPB * 4), r_out = rsrc(idx_out + b0, IPB * 4), r_ang = rsrc(angle + b0, IPB * 2);

    u32x4 I[NGRP], O[NGRP];
    u32x2 A[NGRP];
    u32 It, Ot, At;
    constexpr int G0 = (NGRP + 2) / 3, G1 = (2 * NGRP + 2) / 3;
    auto load_part = [&](int ci, int part) {
        const u32 base = ci < CPB ? ci * CH : 0x10000000u;
        const int k0 = part == 0 ? 0 : part == 1 ? G0 : G1, k1 = part == 0 ? G0 : part == 1 ? G1 : NGRP;
#pragma unroll
        for (int k = 0; k < NGRP; k++) {
            if (k >= k0 && k < k1) {
                const u32 e = base + (t + P1T * k) * 4;
                I[k] = __builtin_amdgcn_raw_buffer_load_b128(r_in, e * 4, 0, CFG_LD_AUX);
                O[k] = __builtin_amdgcn_raw_buffer_load_b128(r_out, e * 4, 0, CFG_LD_AUX);
                A[k] = __builtin_amdgcn_raw_buffer_load_b64(r_ang, e * 2, 0, CFG_LD_AUX);
            }
        }
        if (part == 2) {
            const u32 e = t < TAILMAX ? base + MAINI + t : 0x10000000u;
            It = __builtin_amdgcn_raw_buffer_load_b32(r_in, e * 4, 0, CFG_LD_AUX);
            Ot = __builtin_amdgcn_raw_buffer_load_b32(r_out, e * 4, 0, CFG_LD_AUX);
            At = __builtin_amdgcn_raw_buffer_load_b16(r_ang, e * 2, 0, CFG_LD_AUX);
        }
        __builtin_amdgcn_sched_barrier(0);
    };
    u32 X[NI], Y[NI];
    auto keys = [&]() {
        auto key = [&](int q, u32 in, u32 o, u32 ang) {
            X[q] = (((o >> 8) & ~15u) | (o & 15)) | (in << 12);
            const u32 r = in < R1 ? 0u : in < R2 ? 1u : 2u;
            Y[q] = ang | ((r * NBIN + ((o >> 4) & (NBIN - 1))) << 16);
            asm volatile("" : "+v"(X[q]), "+v"(Y[q]));
        };
        u32 am = At & 0x7fffu;
#pragma unroll
        for (int k = 0; k < NGRP; k++) {
            am = pk_max(am, pk_max(A[k].x & 0x7fff7fffu, A[k].y & 0x7fff7fffu));
#pragma unroll
            for (int j = 0; j < 4; j++) key(k * 4 + j, I[k][j], O[k][j], (j & 1) ? (A[k][j >> 1] >> 16) : (A[k][j >> 1] & 0xffff));
        }
        key(NI - 1, It, Ot, At);
        if ((am & 0x7f80u) == 0x7f80u || (am & 0x7f800000u) == 0x7f800000u) atomicMax(&misc[0], 0x8000u);
        __builtin_amdgcn_sched_barrier(0);
    };
    auto count = [&](bool tail) {
#pragma unroll
        for (int q = 0; q < NI - 1; q++) atomicAdd(&cnt[Y[q] >> 16], 1u);
        if (tail) atomicAdd(&cnt[Y[NI - 1] >> 16], 1u);
    };
    auto ntail = [&](int ci) { return (ci == CPB - 1 ? IPB - (CPB - 1) * CH : CH) - MAINI; };

    load_part(0, 0); load_part(0, 1); load_part(0, 2);
    {
        constexpr int MW = (E_ * 8 / 16 + P1B - 1) / P1B;
        constexpr int NJ = (MW + P1T - 1) / P1T;
        const __amdgpu_buffer_rsrc_t r_m = rsrc(mtab, E_ * 8);
        u32 mx = 0;
#pragma unroll
        for (int j = 0; j < NJ; j++) {
            const int w = t + P1T * j;
            const u32x4 mv = __builtin_amdgcn_raw_buffer_load_b128(r_m, w < MW ? (blockIdx.x * MW + w) * 16 : OOB, 0, 0);
            mx = pk_max(mx, pk_max(pk_max(mv.x & 0x7fff7fffu, mv.y & 0x7fff7fffu), pk_max(mv.z & 0x7fff7fffu, mv.w & 0x7fff7fffu)));
        }
        if (t < 2) misc[t] = 0;
        for (int i = t; i < NKEY; i += P1T) cnt[i] = 0;
        for (int i = t; i < ROWB / 4; i += P1T) tabrow[i] = 0;
        __syncthreads();
        mx = wave_max(max(mx >> 16, mx & 0xffff));
        if ((t & 63) == 63) atomicMax(&misc[0], mx);
    }
    keys();
    load_part(1, 0);
    count(t < ntail(0));
    load_part(1, 1);
#pragma unroll 1
    for (int ci = 0; ci < CPB; ci++) {
        asm volatile("" : "+v"(t));
        const int c = blockIdx.x * CPB + ci;
        const bool tail = t < ntail(ci);
        __syncthreads();
        if (t < 64) {
            u32 sbase = 0;
            u32 pk[NR][2], end[NR];
#pragma unroll
            for (int s = 0; s < NR; s++) {
                const u32x4 h = *(const u32x4*)&cnt[s * NBIN + t * 4];
                *(u32x4*)&cnt[s * NBIN + t * 4] = (u32x4){0, 0, 0, 0};
                const u32x4 un = (h + 1u) >> 1;
                const u32 sum = un.x + un.y + un.z + un.w;
                const u32 inc = wave_scan(sum);
                const u32 tot = __builtin_amdgcn_readlane(inc, 63);
                const u32 p0 = sbase + inc - sum, p1 = p0 + un.x, p2 = p1 + un.y, p3 = p2 + un.z;
                const u32x4 p = {p0, p1, p2, p3};
#pragma unroll
                for (int j = 0; j < 4; j++)
                    if (h[j] & 1) {
                        const u32 q = 2 * p[j] + h[j];
                        ((u32*)U64)[q] = DUMMY | 0xfffff000u;
                        ((u16*)U32)[q] = 0;
                    }
                *(u32x4*)&cur[s * NBIN + t * 4] = p + p;
                pk[s][0] = p0 | (p1 << 16);
                pk[s][1] = p2 | (p3 << 16);
                sbase += tot;
                end[s] = sbase;
            }
            u32* row = tabrow + ci * 2;
            const u32 mhi = misc[0] << 16;
#pragma unroll
            for (int j = 0; j < 4; j++) {
                const u32 o0 = (pk[0][j >> 1] >> (16 * (j & 1))) & 0xffff, o1 = (pk[1][j >> 1] >> (16 * (j & 1))) & 0xffff;
                const u32 o2 = (pk[2][j >> 1] >> (16 * (j & 1))) & 0xffff;
                *(u32x2*)&row[(t * 4 + j) * (CPB * 2)] = (u32x2){o0 | (o1 << 16), o2 | mhi};
            }
            if (t == 0) {
                *(u32x2*)&row[NBIN * (CPB * 2)] = (u32x2){end[0] | (end[1] << 16), end[2] | mhi};
                misc[1] = sbase;
            }
        }
        __syncthreads();
        load_part(ci + 1, 2);
        auto scatter = [&](int q0, int q1) {
            u32 pos[8];
#pragma unroll
            for (int q = q0; q < q1; q++) pos[q - q0] = atomicAdd(&cur[Y[q] >> 16], 1u);
#pragma unroll
            for (int q = q0; q < q1; q++) {
                ((u32*)U64)[pos[q - q0]] = X[q];
                ((u16*)U32)[pos[q - q0]] = (u16)Y[q];
            }
        };
        scatter(0, 8);
        __builtin_amdgcn_sched_barrier(0);
        scatter(8, 16);
        if (tail) scatter(16, 17);
        __syncthreads();
        const u32 total = __builtin_amdgcn_readfirstlane(misc[1]);
        const bool more = ci + 1 < CPB;
        if (more) keys();
        load_part(ci + 2, 0);
        const __amdgpu_buffer_rsrc_t r_rec = rsrc(rec + (size_t)c * NU * 3, (total * 12 + 127) & ~127u);
#pragma unroll
        for (int j = 0; j < (NU + P1T - 1) / P1T; j++) {
            const u32 u = min((u32)(t + P1T * j), (u32)(NU - 1));
            const u64 v = U64[u];
            const u32 w = U32[u];
            __builtin_amdgcn_raw_buffer_store_b96((u32x3){(u32)v, (u32)(v >> 32), w}, r_rec, (t + P1T * j) * 12, 0, CFG_ST_AUX);
        }
        __builtin_amdgcn_sched_barrier(0);
        load_part(ci + 2, 1);
        if (more) count(t < ntail(ci + 1));
    }
    for (int i = t; i < ROWB / 16; i += P1T) ((u32x4*)(tab + (size_t)blockIdx.x * (ROWB / 4)))[i] = ((const u32x4*)tabrow)[i];
}

struct __attribute__((aligned(16))) Desc { u32 lo, hi, base, pad; };
struct P2Shared {
    u64 acc[4][ACCN];
    Desc desc[WIN / 64];
    u32 deg[ACCN];
    u32 delta[NR * NCH];
    u64 wsum[NR][16];
    u32 wmax[16];
    u32 misc[4];
    __attribute__((aligned(16))) float xg[3][64][4];
    __attribute__((aligned(16))) float w[144];
};

__device__ __forceinline__ void accumulate(P2Shared& sh, u32 row, u32 angbits, u32x2 m, float fr0, float fr1, float S) {
    const float a = __uint_as_float(angbits);
    const float c0 = __builtin_amdgcn_cosf(__builtin_amdgcn_fractf(a * fr0)) * S;
    const float c1 = __builtin_amdgcn_cosf(__builtin_amdgcn_fractf(a * fr1)) * S;
    const float m0 = __uint_as_float(m.x << 16), m1 = __uint_as_float(m.x & 0xffff0000u);
    const float m2 = __uint_as_float(m.y << 16), m3 = __uint_as_float(m.y & 0xffff0000u);
    const float K = 12582912.0f;
    const u32 t0 = __float_as_uint(__builtin_fmaf(c0, m0, K)), t1 = __float_as_uint(__builtin_fmaf(c0, m1, K));
    const u32 t2 = __float_as_uint(__builtin_fmaf(c0, m2, K)), t3 = __float_as_uint(__builtin_fmaf(c0, m3, K));
    const u32 t4 = __float_as_uint(__builtin_fmaf(c1, m0, K)), t5 = __float_as_uint(__builtin_fmaf(c1, m1, K));
    const u32 t6 = __float_as_uint(__builtin_fmaf(c1, m2, K)), t7 = __float_as_uint(__builtin_fmaf(c1, m3, K));
    atomicAdd(&sh.acc[0][row], ((u64)t1 << 32) | t0);
    atomicAdd(&sh.acc[1][row], ((u64)t3 << 32) | t2);
    atomicAdd(&sh.acc[2][row], ((u64)t5 << 32) | t4);
    atomicAdd(&sh.acc[3][row], ((u64)t7 << 32) | t6);
    atomicAdd(&sh.deg[row], 1u);
}
__device__ __forceinline__ void accumulate_wide(double* __restrict__ wide, u32 row, u32 angbits, u32x2 mm, float fr0, float fr1) {
    if (row >= (u32)EPB) return;
    const float a = __uint_as_float(angbits);
    const float c0 = __builtin_amdgcn_cosf(__builtin_amdgcn_fractf(a * fr0));
    const float c1 = __builtin_amdgcn_cosf(__builtin_amdgcn_fractf(a * fr1));
    const float m[4] = {__uint_as_float(mm.x << 16), __uint_as_float(mm.x & 0xffff0000u), __uint_as_float(mm.y << 16), __uint_as_float(mm.y & 0xffff0000u)};
#pragma unroll
    for (int k = 0; k < 4; k++) {
        atomicAdd(&wide[row * 8 + k], (double)(c0 * m[k]));
        atomicAdd(&wide[row * 8 + 4 + k], (double)(c1 * m[k]));
    }
}

__global__ __launch_bounds__(1024, 4) void p2_kernel(const u32* __restrict__ rec, const u32* __restrict__ tab,
                                                     const u32* __restrict__ mtab, const u16* __restrict__ dist,
                                                     const u16* __restrict__ centers, const u16* __restrict__ ang_freq,
                                                     const u16* __restrict__ w_rbf, const u16* __restrict__ w_sbf,
                                                     const u16* __restrict__ w_down, const u16* __restrict__ w_up,
                                                     double* __restrict__ wide_all, u32* __restrict__ out) {
    __shared__ __attribute__((aligned(16))) P2Shared sh;
    int t = threadIdx.x;
    const int lane = t & 63;
    const int wave = __builtin_amdgcn_readfirstlane(t >> 6);
    const int bin = (blockIdx.x & 7) * (NBIN / 8) + (blockIdx.x >> 3);
    const __amdgpu_buffer_rsrc_t r_rec = rsrc(rec, (u32)WS_REC_BYTES), r_tab = rsrc(tab, (u32)WS_TAB_BYTES);
    const __amdgpu_buffer_rsrc_t r_m = rsrc(mtab, E_ * 8), r_dist = rsrc(dist, E_ * 2), r_res = rsrc(out, E_ * 8);
    double* wide = wide_all + (size_t)bin * EPB * 8;

    u32x2 e0, e1;
    auto load_table = [&]() {
        const u32 b = t / CPB, ci = t - b * CPB;
        const u32 off = t < NCH ? b * ROWB + (bin * CPB + ci) * 8 : OOB - CPB * 8;
        e0 = __builtin_amdgcn_raw_buffer_load_b64(r_tab, off, 0, 0);
        e1 = __builtin_amdgcn_raw_buffer_load_b64(r_tab, off + CPB * 8, 0, 0);
    };
    load_table();
    auto edge = [&](u32 el) -> u32 { return ((el >> 4) << 12) | (bin << 4) | (el & 15); };
    u32 dv4[4];
#pragma unroll
    for (int i = 0; i < 4; i++) dv4[i] = __builtin_amdgcn_raw_buffer_load_b16(r_dist, (t + 1024 * i < EPB) ? edge(t + 1024 * i) * 2 : OOB, 0, 0);
    auto wload = [&](const u16* p, int first, int n) -> u32 { return __builtin_amdgcn_raw_buffer_load_b16(rsrc(p, n * 2), (u32)(t - first) * 2, 0, 0); };
    const u32 wv0 = wload(w_sbf, 0, 64), wv1 = wload(w_rbf, 64, 32), wv2 = wload(w_down, 96, 16), wv3 = wload(w_up, 112, 16), wv4 = wload(centers, 128, 8);
    const u32 fbits = *(const u32*)ang_freq;
    const float fr0 = __uint_as_float(fbits << 16) * 0.15915494309189535f, fr1 = __uint_as_float(fbits & 0xffff0000u) * 0.15915494309189535f;
    __builtin_amdgcn_sched_barrier(0);
    for (int i = t; i < 4 * ACCN / 2; i += 1024) ((u32x4*)sh.acc)[i] = (u32x4){0, 0, 0, 0};
    for (int i = t; i < ACCN; i += 1024) sh.deg[i] = 0;
    if (t < WIN / 64) *(u32x4*)&sh.desc[t] = (u32x4){0, 0, 0, 0};
    if (t < 3) sh.misc[t] = t == 2 ? 32 : 0;
    __builtin_amdgcn_sched_barrier(0);
    if (t < 136) sh.w[t] = bf2f(wv0 | wv1 | wv2 | wv3 | wv4);
    {
        const u32 mx = wave_max(e0.y >> 16);
        if (lane == 63) sh.wmax[wave] = mx;
    }

    bool slow = false;
    float S = 1.0f, invS = 1.0f;
    u32 mbits = 0;
    u32 win = 0;
#pragma unroll 1
    for (;;) {
        asm volatile("" : "+v"(t));
        u32 len[NR], st[NR], inc[NR], ninc[NR];
        st[0] = e0.x & 0xffff; st[1] = e0.x >> 16; st[2] = e0.y & 0xffff;
        len[0] = (e1.x & 0xffff) - st[0]; len[1] = (e1.x >> 16) - st[1]; len[2] = (e1.y & 0xffff) - st[2];
#pragma unroll
        for (int s = 0; s < NR; s++) {
            inc[s] = wave_scan(len[s]);
            const u64 ne = __ballot(len[s] != 0);
            ninc[s] = __builtin_amdgcn_mbcnt_hi((u32)(ne >> 32), __builtin_amdgcn_mbcnt_lo((u32)ne, 0));
            const u32 wt = __builtin_amdgcn_readlane(inc[s], 63);
            if (lane == 0) sh.wsum[s][wave] = (u64)wt | ((u64)__popcll(ne) << 32);
        }
        u32 warm = 0;
        if (win == 0 && !slow) {
            constexpr u32 lines = (R1 * 8 / 128 + 31) / 32;
            warm = __builtin_amdgcn_raw_buffer_load_b32(r_m, (u32)t < lines ? ((blockIdx.x >> 3) * lines + t) * 128 : OOB, 0, 0);
        }
        __syncthreads();
        if (win == 0 && !slow) {
            mbits = __builtin_amdgcn_readlane(wave_max(lane < 16 ? sh.wmax[lane] : 0), 63);
            const int ex = max((int)((mbits >> 7) & 0xff) - 127 + 1, -100);
            S = __uint_as_float((u32)(22 - ex + 127) << 23);
            invS = __uint_as_float((u32)(127 - 22 + ex) << 23);
            if ((fbits & 0x7f80u) == 0x7f80u || (fbits & 0x7f800000u) == 0x7f800000u) mbits = 0x8000u;
        }
        const u32 W0 = win * WIN;
        u32 L = 0, nseg = 0;
#pragma unroll
        for (int s = 0; s < NR; s++) {
            const u64 x = lane < 16 ? sh.wsum[s][lane] : 0;
            const u32 sl = wave_scan((u32)x), sn = wave_scan((u32)(x >> 32));
            const u32 P = L + __builtin_amdgcn_readlane(sl - (u32)x, wave) + inc[s] - len[s];
            const u32 ord = nseg + __builtin_amdgcn_readlane(sn - (u32)(x >> 32), wave) + ninc[s];
            L += __builtin_amdgcn_readlane(sl, 63);
            nseg += __builtin_amdgcn_readlane(sn, 63);
            if (len[s]) {
                sh.delta[ord] = (u32)t * NU + st[s] - P;
                const u32 p = P - W0;
                if (p < WIN) atomicOr((p & 32) ? &sh.desc[p >> 6].hi : &sh.desc[p >> 6].lo, 1u << (p & 31));
                const u32 end = P + len[s];
                if (end >= W0 && P < W0 + WIN) {
                    u32 w = P < W0 ? 0 : (p >> 6) + 1;
                    const u32 w1 = min((end - W0) >> 6, (u32)(WIN / 64 - 1));
                    for (; w <= w1; w++) sh.desc[w].base = ord + 1;
                }
            }
        }
        const u32 nwin = (L + WIN - 1) / WIN;
        __syncthreads();

        const u32 rem = L > W0 ? min(L - W0, (u32)WIN) : 0;
        const u32 nit = (rem + 63) >> 6;

        auto fetch = [&](u32 k) -> u32x3 {
            const u32x4 d = *(const u32x4*)&sh.desc[min(k, (u32)(WIN / 64 - 1))];
            const u32 lo = __builtin_amdgcn_readfirstlane(d.x), hi = __builtin_amdgcn_readfirstlane(d.y), b = __builtin_amdgcn_readfirstlane(d.z);
            const u64 w1 = (((u64)hi << 32) | lo) >> 1;
            const u32 s = b + (lo & 1) - 1 + __builtin_amdgcn_mbcnt_hi((u32)(w1 >> 32), __builtin_amdgcn_mbcnt_lo((u32)w1, 0));
            const u32 i = k * 64 + lane;
            const bool ok = i < rem;
            const u32 dd = sh.delta[ok ? s : 0];
            return __builtin_amdgcn_raw_buffer_load_b96(r_rec, ok ? (dd + W0 + i) * 12 : OOB, 0, 0);
        };
        auto gather = [&](const u32x3& r, u32 k, u32x2& g0, u32x2& g1) {
            const bool ok = k * 64 + lane < rem;
            g0 = __builtin_amdgcn_raw_buffer_load_b64(r_m, ok ? (r.x >> 12) * 8 : OOB, 0, 0);
            g1 = __builtin_amdgcn_raw_buffer_load_b64(r_m, ok ? (r.y >> 12) * 8 : OOB, 0, 0);
        };
        auto ticket = [&]() -> u32 { u32 v = 0; if (lane == 0) v = atomicAdd(&sh.misc[2], 1u); return v; };

        float G[3][8], rw[3][4];
        u32x2 mm[3];
        auto edge_terms = [&](auto NEc, auto I0c, auto& Go, auto& rwo) {
            constexpr int NE = decltype(NEc)::value, I0 = decltype(I0c)::value;
#pragma unroll
            for (int i = 0; i < NE; i++) {
#pragma unroll
                for (int q = 0; q < 8; q++) Go[i][q] = 0;
#pragma unroll
                for (int q = 0; q < 4; q++) rwo[i][q] = 0;
            }
#pragma unroll 1
            for (int r = 0; r < 8; r++) {
                const f32x4 w0 = *(const f32x4*)&sh.w[r * 8], w1 = *(const f32x4*)&sh.w[r * 8 + 4], w2 = *(const f32x4*)&sh.w[64 + r * 4];
                const float cr = sh.w[128 + r];
#pragma unroll
                for (int i = 0; i < NE; i++) {
                    const float x = bf2f(dv4[I0 + i]) - cr;
                    const float rb = __builtin_amdgcn_exp2f(-14.426950408889634f * x * x);
#pragma unroll
                    for (int q = 0; q < 4; q++) { Go[i][q] += rb * w0[q]; Go[i][4 + q] += rb * w1[q]; rwo[i][q] += rb * w2[q]; }
                }
            }
        };
        auto all_edge_terms = [&]() {
            if (wave == 0) {
                float Gx[1][8], rwx[1][4];
                edge_terms(std::integral_constant<int, 1>{}, std::integral_constant<int, 3>{}, Gx, rwx);
                *(f32x4*)&sh.xg[0][lane][0] = (f32x4){Gx[0][0], Gx[0][1], Gx[0][2], Gx[0][3]};
                *(f32x4*)&sh.xg[1][lane][0] = (f32x4){Gx[0][4], Gx[0][5], Gx[0][6], Gx[0][7]};
                *(f32x4*)&sh.xg[2][lane][0] = (f32x4){rwx[0][0], rwx[0][1], rwx[0][2], rwx[0][3]};
            }
            edge_terms(std::integral_constant<int, 3>{}, std::integral_constant<int, 0>{}, G, rw);
        };
        auto finish = [&](auto NEc, auto I0c, bool wide_sums) -> bool {
            constexpr int NE = decltype(NEc)::value, I0 = decltype(I0c)::value;
            bool bad = false;
#pragma unroll
            for (int i = 0; i < NE; i++) {
                asm volatile("" ::: "memory");
                const int el = t + 1024 * (I0 + i);
                float Aa[8];
                if (wide_sums) {
#pragma unroll
                    for (int q = 0; q < 8; q++) Aa[q] = (float)__hip_atomic_load(&wide[el * 8 + q], __ATOMIC_RELAXED, __HIP_MEMORY_SCOPE_AGENT);
                } else {
                    const u32 dg = sh.deg[el];
                    bad |= dg > MAXDEG;
                    const u64 kk = (u64)dg * 0x4B4000004B400000ull;
#pragma unroll
                    for (int q = 0; q < 4; q++) {
                        const u64 v = sh.acc[q][el] - kk;
                        const int lo = (int)(u32)v;
                        const int hi = (int)((u32)(v >> 32) - (u32)(lo >> 31));
                        Aa[2 * q] = (float)lo * invS;
                        Aa[2 * q + 1] = (float)hi * invS;
                    }
                }
                f32x4 b0 = {0, 0, 0, 0}, b1 = {0, 0, 0, 0}, u = {0, 0, 0, 0};
#pragma unroll
                for (int k = 0; k < 4; k++) { const f32x4 wd = *(const f32x4*)&sh.w[96 + k * 4]; b0 += Aa[k] * wd; b1 += Aa[4 + k] * wd; }
#pragma unroll
                for (int k = 0; k < 4; k++) u += (G[i][k] * b0[k] + G[i][4 + k] * b1[k]) * *(const f32x4*)&sh.w[112 + k * 4];
                const float o0 = __uint_as_float(mm[i].x << 16) + u[0] * rw[i][0], o1 = __uint_as_float(mm[i].x & 0xffff0000u) + u[1] * rw[i][1];
                const float o2 = __uint_as_float(mm[i].y << 16) + u[2] * rw[i][2], o3 = __uint_as_float(mm[i].y & 0xffff0000u) + u[3] * rw[i][3];
                __builtin_amdgcn_raw_buffer_store_b64((u32x2){pack_bf16(o0, o1), pack_bf16(o2, o3)}, r_res, edge(el) * 8, 0, 0);
            }
            return bad;
        };

        auto mainloop = [&](auto SLOWc) {
            constexpr bool SLOW = decltype(SLOWc)::value;
            u32x3 cur, nxt;
            u32 kc = wave, kn = wave + 16, tk = 0;
            const bool any = kc < nit;
            if (any) {
                tk = ticket();
                cur = fetch(kc);
                nxt = fetch(kn);
            }
            __builtin_amdgcn_sched_barrier(0);
            all_edge_terms();
            asm volatile("" :: "v"(warm));
            __builtin_amdgcn_sched_barrier(0);
            if (any) {
                u32x2 c0, c1;
                gather(cur, kc, c0, c1);
                while (kc < nit) {
                    u32x2 n0, n1;
                    gather(nxt, kn, n0, n1);
                    const u32 knn = __builtin_amdgcn_readfirstlane(tk);
                    tk = ticket();
                    const u32x3 nn = fetch(knn);
                    __builtin_amdgcn_sched_barrier(0);
                    if (kc * 64 + lane < rem) {
                        if (SLOW) {
                            accumulate_wide(wide, cur.x & 0xfff, cur.z << 16, c0, fr0, fr1);
                            accumulate_wide(wide, cur.y & 0xfff, cur.z & 0xffff0000u, c1, fr0, fr1);
                        } else {
                            accumulate(sh, cur.x & 0xfff, cur.z << 16, c0, fr0, fr1, S);
                            accumulate(sh, cur.y & 0xfff, cur.z & 0xffff0000u, c1, fr0, fr1, S);
                        }
                    }
                    __builtin_amdgcn_sched_barrier(0);
                    cur = nxt; nxt = nn; c0 = n0; c1 = n1; kc = kn; kn = knn;
                }
            }
        };
        if (slow) { mainloop(std::true_type{}); asm volatile("s_waitcnt vmcnt(0)" ::: "memory"); }
        else mainloop(std::false_type{});

        win++;
        if (win >= nwin) {
#pragma unroll
            for (int i = 0; i < 3; i++) mm[i] = __builtin_amdgcn_raw_buffer_load_b64(r_m, edge(t + 1024 * i) * 8, 0, 0);
            const u32x2 mmx = __builtin_amdgcn_raw_buffer_load_b64(r_m, t < EPB - 3072 ? edge(t + 3072) * 8 : OOB, 0, 0);
            __syncthreads();
            if (slow) __threadfence();
            bool bad = finish(std::integral_constant<int, 3>{}, std::integral_constant<int, 0>{}, slow);
            if (wave == 0) {
                const f32x4 g0 = *(const f32x4*)&sh.xg[0][lane][0], g1 = *(const f32x4*)&sh.xg[1][lane][0], g2 = *(const f32x4*)&sh.xg[2][lane][0];
#pragma unroll
                for (int q = 0; q < 4; q++) { G[0][q] = g0[q]; G[0][4 + q] = g1[q]; rw[0][q] = g2[q]; }
                mm[0] = mmx;
                bad |= finish(std::integral_constant<int, 1>{}, std::integral_constant<int, 3>{}, slow);
            }
            if (slow) break;
            if (bad || mbits >= 0x7f80) sh.misc[1] = 1;
            __syncthreads();
            if (!sh.misc[1]) break;
            slow = true;
            win = 0;
            for (int i = t; i < EPB * 8; i += 1024) __hip_atomic_store(&wide[i], 0.0, __ATOMIC_RELAXED, __HIP_MEMORY_SCOPE_AGENT);
            __threadfence();
        } else {
            __syncthreads();
        }
        load_table();
        if (t < WIN / 64) *(u32x4*)&sh.desc[t] = (u32x4){0, 0, 0, 0};
        if (t == 0) sh.misc[2] = 32;
        __syncthreads();
    }
}

extern "C" void kernel_launch(void* const* d_in, const int* in_sizes, int n_in, void* d_out, int out_size, void* d_ws, size_t ws_size,
                              hipStream_t stream) {
    const u32* m = (const u32*)d_in[0];
    const u32* idx_in = (const u32*)d_in[1];
    const u32* idx_out = (const u32*)d_in[2];
    const u16* dist = (const u16*)d_in[3];
    const u16* angle = (const u16*)d_in[4];
    const u16* centers = (const u16*)d_in[5];
    const u16* ang_freq = (const u16*)d_in[6];
    const u16* w_rbf = (const u16*)d_in[7];
    const u16* w_sbf = (const u16*)d_in[8];
    const u16* w_down = (const u16*)d_in[9];
    const u16* w_up = (const u16*)d_in[10];
    char* ws = (char*)d_ws;
    u32* rec = (u32*)(ws + WS_REC);
    u32* tab = (u32*)(ws + WS_TAB);
    p1_kernel<<<P1B, P1T, 0, stream>>>(idx_in, idx_out, angle, m, rec, tab);
    p2_kernel<<<NBIN, 1024, 0, stream>>>(rec, tab, m, dist, centers, ang_freq, w_rbf, w_sbf, w_down, w_up, (double*)(ws + WS_WIDE), (u32*)d_out);
}
```

```cpp
#include <hip/hip_runtime.h>
#include <stdint.h>
#include <type_traits>

typedef unsigned int u32;
typedef unsigned short u16;
typedef unsigned long long u64;
typedef u32 u32x4 __attribute__((ext_vector_type(4)));
typedef u32 u32x3 __attribute__((ext_vector_type(3)));
typedef u32 u32x2 __attribute__((ext_vector_type(2)));
typedef u16 u16x2 __attribute__((ext_vector_type(2)));
typedef float f32x4 __attribute__((ext_vector_type(4)));
typedef __bf16 bf16x2 __attribute__((ext_vector_type(2)));

#define CFG_LD_AUX 2
#define CFG_ST_AUX 16
#define CFG_P0 2
#define CFG_P1 2
#define CFG_P2 0
#define CFG_P3 1
#define CFG_PT 1
#define CFG_R1 266667
#define CFG_R2 533334

constexpr int E_ = 800000, T_ = 12800000;
constexpr u32 OOB = 0xfffffff0u;

constexpr int NBIN = 256;
constexpr int EPB = 3136;
constexpr int ACCN = 3144;
constexpr u32 DUMMY = EPB;
constexpr int NR = 3;
constexpr u32 R1 = CFG_R1, R2 = CFG_R2;
constexpr int NKEY = NBIN * NR;

constexpr int P1T = 1024, P1B = 256, CPB = 3, CH = 16668;
constexpr int IPB = T_ / P1B;
constexpr int NGRP = 4;
constexpr int MAINI = P1T * NGRP * 4;
constexpr int TAILMAX = CH - MAINI;
constexpr int NI = NGRP * 4 + 1;
constexpr int NCH = P1B * CPB;
static_assert(TAILMAX > 0 && TAILMAX <= P1T && IPB - (CPB - 1) * CH > MAINI && IPB * P1B == T_ && CH % 4 == 0 && IPB % 4 == 0, "chunks");
constexpr int NU = (CH / 2 + NKEY / 2 + 8 + 31) & ~31;
constexpr int ROWB = ((NBIN + 1) * CPB * 8 + 127) & ~127;
static_assert(ROWB / 16 <= P1T, "table row");
static_assert(NU < 65536 && NCH < 1024, "table offsets are 16 bits, segment counts 10 bits");

constexpr int WIN = 32768;
constexpr u32 MAXDEG = 512;

constexpr size_t WS_REC = 0;
constexpr size_t WS_REC_BYTES = (size_t)NCH * NU * 12;
constexpr size_t WS_TAB = WS_REC + WS_REC_BYTES;
constexpr size_t WS_TAB_BYTES = (size_t)P1B * ROWB;
constexpr size_t WS_WIDE = WS_TAB + WS_TAB_BYTES;
constexpr size_t WS_WIDE_BYTES = (size_t)NBIN * EPB * 64;
static_assert(WS_WIDE % 128 == 0 && WS_WIDE + WS_WIDE_BYTES <= ((size_t)256 << 20), "workspace");

__device__ __forceinline__ float bf2f(u32 x) { return __uint_as_float(x << 16); }
__device__ __forceinline__ u32 pack_bf16(float a, float b) { const bf16x2 v = {(__bf16)a, (__bf16)b}; return __builtin_bit_cast(u32, v); }
__device__ __forceinline__ __amdgpu_buffer_rsrc_t rsrc(const void* p, u32 bytes) {
    return __builtin_amdgcn_make_buffer_rsrc((void*)p, 0, bytes, 0x00020000);
}
__device__ __forceinline__ u32 wave_scan(u32 v) {
    v += (u32)__builtin_amdgcn_update_dpp(0, (int)v, 0x111, 0xf, 0xf, true);
    v += (u32)__builtin_amdgcn_update_dpp(0, (int)v, 0x112, 0xf, 0xf, true);
    v += (u32)__builtin_amdgcn_update_dpp(0, (int)v, 0x114, 0xf, 0xf, true);
    v += (u32)__builtin_amdgcn_update_dpp(0, (int)v, 0x118, 0xf, 0xf, true);
    v += (u32)__builtin_amdgcn_update_dpp(0, (int)v, 0x142, 0xa, 0xf, false);
    v += (u32)__builtin_amdgcn_update_dpp(0, (int)v, 0x143, 0xc, 0xf, false);
    return v;
}
__device__ __forceinline__ u32 row_scan(u32 v) {
    v += (u32)__builtin_amdgcn_update_dpp(0, (int)v, 0x111, 0xf, 0xf, true);
    v += (u32)__builtin_amdgcn_update_dpp(0, (int)v, 0x112, 0xf, 0xf, true);
    v += (u32)__builtin_amdgcn_update_dpp(0, (int)v, 0x114, 0xf, 0xf, true);
    v += (u32)__builtin_amdgcn_update_dpp(0, (int)v, 0x118, 0xf, 0xf, true);
    return v;
}
__device__ __forceinline__ u32 wave_max(u32 v) {
    v = max(v, (u32)__builtin_amdgcn_update_dpp(0, (int)v, 0x111, 0xf, 0xf, true));
    v = max(v, (u32)__builtin_amdgcn_update_dpp(0, (int)v, 0x112, 0xf, 0xf, true));
    v = max(v, (u32)__builtin_amdgcn_update_dpp(0, (int)v, 0x114, 0xf, 0xf, true));
    v = max(v, (u32)__builtin_amdgcn_update_dpp(0, (int)v, 0x118, 0xf, 0xf, true));
    v = max(v, (u32)__builtin_amdgcn_update_dpp(0, (int)v, 0x142, 0xa, 0xf, false));
    v = max(v, (u32)__builtin_amdgcn_update_dpp(0, (int)v, 0x143, 0xc, 0xf, false));
    return v;
}
__device__ __forceinline__ u32 pk_max(u32 a, u32 b) {
    return __builtin_bit_cast(u32, __builtin_elementwise_max(__builtin_bit_cast(u16x2, a), __builtin_bit_cast(u16x2, b)));
}

__global__ __launch_bounds__(P1T, 4) void p1_kernel(const u32* __restrict__ idx_in, const u32* __restrict__ idx_out,
                                                    const u16* __restrict__ angle, const u32* __restrict__ mtab,
                                                    u32* __restrict__ rec, u32* __restrict__ tab) {
    __shared__ __attribute__((aligned(16))) u32 stage[NU * 3];
    __shared__ __attribute__((aligned(16))) u32 cnt[NKEY];
    __shared__ __attribute__((aligned(16))) u32 cur[NKEY];
    __shared__ __attribute__((aligned(16))) u32 tabrow[ROWB / 4];
    __shared__ u32 misc[3];
    int t = threadIdx.x;
    const int b0 = blockIdx.x * IPB;
    const __amdgpu_buffer_rsrc_t r_in = rsrc(idx_in + b0, IPB * 4), r_out = rsrc(idx_out + b0, IPB * 4), r_ang = rsrc(angle + b0, IPB * 2);

    u32x4 I[NGRP], O[NGRP];
    u32x2 A[NGRP];
    u32 It, Ot, At;
    constexpr int PT[NGRP + 1] = {CFG_P0, CFG_P1, CFG_P2, CFG_P3, CFG_PT};
    auto load_at = [&](int ci, int point) {
        const u32 base = ci < CPB ? ci * CH : 0x10000000u;
#pragma unroll
        for (int k = 0; k < NGRP; k++) {
            if (point < 0 || PT[k] == point) {
                const u32 e = base + (t + P1T * k) * 4;
                I[k] = __builtin_amdgcn_raw_buffer_load_b128(r_in, e * 4, 0, CFG_LD_AUX);
                O[k] = __builtin_amdgcn_raw_buffer_load_b128(r_out, e * 4, 0, CFG_LD_AUX);
                A[k] = __builtin_amdgcn_raw_buffer_load_b64(r_ang, e * 2, 0, CFG_LD_AUX);
            }
        }
        if (point < 0 || PT[NGRP] == point) {
            const u32 e = t < TAILMAX ? base + MAINI + t : 0x10000000u;
            It = __builtin_amdgcn_raw_buffer_load_b32(r_in, e * 4, 0, CFG_LD_AUX);
            Ot = __builtin_amdgcn_raw_buffer_load_b32(r_out, e * 4, 0, CFG_LD_AUX);
            At = __builtin_amdgcn_raw_buffer_load_b16(r_ang, e * 2, 0, CFG_LD_AUX);
        }
        __builtin_amdgcn_sched_barrier(0);
    };
    u32 X[NI], Y[NI];
    auto keys = [&]() {
        auto key = [&](int q, u32 in, u32 o, u32 ang) {
            X[q] = (((o >> 8) & ~15u) | (o & 15)) | (in << 12);
            const u32 r = in < R1 ? 0u : in < R2 ? 1u : 2u;
            Y[q] = ang | ((r * NBIN + ((o >> 4) & (NBIN - 1))) << 16);
            asm volatile("" : "+v"(X[q]), "+v"(Y[q]));
        };
        u32 am = At & 0x7fffu;
#pragma unroll
        for (int k = 0; k < NGRP; k++) {
            am = pk_max(am, pk_max(A[k].x & 0x7fff7fffu, A[k].y & 0x7fff7fffu));
#pragma unroll
            for (int j = 0; j < 4; j++) key(k * 4 + j, I[k][j], O[k][j], (j & 1) ? (A[k][j >> 1] >> 16) : (A[k][j >> 1] & 0xffff));
        }
        key(NI - 1, It, Ot, At);
        if ((am & 0x7f80u) == 0x7f80u || (am & 0x7f800000u) == 0x7f800000u) atomicMax(&misc[0], 0x8000u);
        __builtin_amdgcn_sched_barrier(0);
    };
    auto count = [&](bool tail) {
#pragma unroll
        for (int q = 0; q < NI - 1; q++) atomicAdd(&cnt[Y[q] >> 16], 1u);
        if (tail) atomicAdd(&cnt[Y[NI - 1] >> 16], 1u);
    };
    auto ntail = [&](int ci) { return (ci == CPB - 1 ? IPB - (CPB - 1) * CH : CH) - MAINI; };

    load_at(0, -1);
    {
        constexpr int MW = (E_ * 8 / 16 + P1B - 1) / P1B;
        constexpr int NJ = (MW + P1T - 1) / P1T;
        const __amdgpu_buffer_rsrc_t r_m = rsrc(mtab, E_ * 8);
        u32 mx = 0, mn = 0;
#pragma unroll
        for (int j = 0; j < NJ; j++) {
            const int w = t + P1T * j;
            const u32x4 mv = __builtin_amdgcn_raw_buffer_load_b128(r_m, w < MW ? (blockIdx.x * MW + w) * 16 : OOB, 0, 0);
            const u32 pa = pk_max(mv.x & 0x7fff7fffu, mv.y & 0x7fff7fffu), pb = pk_max(mv.z & 0x7fff7fffu, mv.w & 0x7fff7fffu);
            const u32 ra = max(pa >> 16, pa & 0xffff), rb = max(pb >> 16, pb & 0xffff);
            mx = max(mx, max(ra, rb));
            mn = max(mn, max(ra ? 0x7fff - ra : 0u, rb ? 0x7fff - rb : 0u));
        }
        if (t < 3) misc[t] = 0;
        for (int i = t; i < NKEY; i += P1T) cnt[i] = 0;
        for (int i = t; i < ROWB / 4; i += P1T) tabrow[i] = 0;
        __syncthreads();
        mx = wave_max(mx);
        mn = wave_max(mn);
        if ((t & 63) == 63) { atomicMax(&misc[0], mx); atomicMax(&misc[2], mn); }
    }
    keys();
    load_at(1, 2);
    count(t < ntail(0));
    load_at(1, 3);
#pragma unroll 1
    for (int ci = 0; ci < CPB; ci++) {
        asm volatile("" : "+v"(t));
        const int c = blockIdx.x * CPB + ci;
        const bool tail = t < ntail(ci);
        __syncthreads();
        if (t < 64) {
            u32 sbase = 0;
            u32 pk[NR][2], end[NR];
#pragma unroll
            for (int s = 0; s < NR; s++) {
                const u32x4 h = *(const u32x4*)&cnt[s * NBIN + t * 4];
                *(u32x4*)&cnt[s * NBIN + t * 4] = (u32x4){0, 0, 0, 0};
                const u32x4 un = (h + 1u) >> 1;
                const u32 sum = un.x + un.y + un.z + un.w;
                const u32 inc = wave_scan(sum);
                const u32 tot = __builtin_amdgcn_readlane(inc, 63);
                const u32 p0 = sbase + inc - sum, p1 = p0 + un.x, p2 = p1 + un.y, p3 = p2 + un.z;
                const u32x4 p = {p0, p1, p2, p3};
#pragma unroll
                for (int j = 0; j < 4; j++)
                    if (h[j] & 1) {
                        const u32 q = (p[j] + (h[j] >> 1)) * 3;
                        stage[q + 1] = DUMMY | 0xfffff000u;
                        ((u16*)stage)[q * 2 + 5] = 0;
                    }
                *(u32x4*)&cur[s * NBIN + t * 4] = p + p;
                pk[s][0] = p0 | (p1 << 16);
                pk[s][1] = p2 | (p3 << 16);
                sbase += tot;
                end[s] = sbase;
            }
            u32* row = tabrow + ci * 2;
            const u32 mhi = ((misc[0] >= 0x7f80 ? 255u : misc[0] >> 7) | ((255 - ((0x7fff - misc[2]) >> 7)) << 8)) << 16;
#pragma unroll
            for (int j = 0; j < 4; j++) {
                const u32 o0 = (pk[0][j >> 1] >> (16 * (j & 1))) & 0xffff, o1 = (pk[1][j >> 1] >> (16 * (j & 1))) & 0xffff;
                const u32 o2 = (pk[2][j >> 1] >> (16 * (j & 1))) & 0xffff;
                *(u32x2*)&row[(t * 4 + j) * (CPB * 2)] = (u32x2){o0 | (o1 << 16), o2 | mhi};
            }
            if (t == 0) {
                *(u32x2*)&row[NBIN * (CPB * 2)] = (u32x2){end[0] | (end[1] << 16), end[2] | mhi};
                misc[1] = sbase;
            }
        }
        __syncthreads();
        load_at(ci + 1, 0);
        auto scatter = [&](int q0, int q1) {
            u32 pos[8];
#pragma unroll
            for (int q = q0; q < q1; q++) pos[q - q0] = atomicAdd(&cur[Y[q] >> 16], 1u);
#pragma unroll
            for (int q = q0; q < q1; q++) {
                const u32 base = __umul24(pos[q - q0] >> 1, 12), slot = pos[q - q0] & 1;
                *(u32*)((char*)stage + base + slot * 4) = X[q];
                *(u16*)((char*)stage + base + slot * 2 + 8) = (u16)Y[q];
            }
        };
        scatter(0, 8);
        __builtin_amdgcn_sched_barrier(0);
        load_at(ci + 1, 1);
        scatter(8, 16);
        if (tail) scatter(16, 17);
        __syncthreads();
        const u32 total = __builtin_amdgcn_readfirstlane(misc[1]);
        const bool more = ci + 1 < CPB;
        if (more) keys();
        load_at(ci + 2, 2);
        const __amdgpu_buffer_rsrc_t r_rec = rsrc(rec + (size_t)c * NU * 3, (total * 12 + 127) & ~127u);
#pragma unroll
        for (int j = 0; j < (NU * 12 / 16 + P1T - 1) / P1T; j++) {
            const u32 u = min((u32)(t + P1T * j), (u32)(NU * 12 / 16 - 1));
            __builtin_amdgcn_raw_buffer_store_b128(((const u32x4*)stage)[u], r_rec, (t + P1T * j) * 16, 0, CFG_ST_AUX);
        }
        __builtin_amdgcn_sched_barrier(0);
        load_at(ci + 2, 3);
        if (more) count(t < ntail(ci + 1));
    }
    if (t < ROWB / 16) __builtin_amdgcn_raw_buffer_store_b128(((const u32x4*)tabrow)[t], rsrc(tab + (size_t)blockIdx.x * (ROWB / 4), ROWB), t * 16, 0, CFG_ST_AUX);
}

struct __attribute__((aligned(16))) Desc { u32 lo, hi, base, pad; };
struct P2Shared {
    u64 acc[4][ACCN];
    Desc desc[WIN / 64];
    u32 deg[ACCN];
    u32 delta[NR * NCH];
    u32 wsum[NR + 1][16];
    u32 wmax[2][16];
    u32 misc[4];
    __attribute__((aligned(16))) float xg[3][64][4];
    __attribute__((aligned(16))) float w[144];
};

__device__ __forceinline__ void accumulate(P2Shared& sh, u32 row, u32 angbits, u32x2 m, float fr0, float fr1, float S) {
    const float a = __uint_as_float(angbits);
    const float c0 = __builtin_amdgcn_cosf(__builtin_amdgcn_fractf(a * fr0)) * S;
    const float c1 = __builtin_amdgcn_cosf(__builtin_amdgcn_fractf(a * fr1)) * S;
    const float m0 = __uint_as_float(m.x << 16), m1 = __uint_as_float(m.x & 0xffff0000u);
    const float m2 = __uint_as_float(m.y << 16), m3 = __uint_as_float(m.y & 0xffff0000u);
    const float K = 12582912.0f;
    const u32 t0 = __float_as_uint(__builtin_fmaf(c0, m0, K)), t1 = __float_as_uint(__builtin_fmaf(c0, m1, K));
    const u32 t2 = __float_as_uint(__builtin_fmaf(c0, m2, K)), t3 = __float_as_uint(__builtin_fmaf(c0, m3, K));
    const u32 t4 = __float_as_uint(__builtin_fmaf(c1, m0, K)), t5 = __float_as_uint(__builtin_fmaf(c1, m1, K));
    const u32 t6 = __float_as_uint(__builtin_fmaf(c1, m2, K)), t7 = __float_as_uint(__builtin_fmaf(c1, m3, K));
    atomicAdd(&sh.acc[0][row], ((u64)t1 << 32) | t0);
    atomicAdd(&sh.acc[1][row], ((u64)t3 << 32) | t2);
    atomicAdd(&sh.acc[2][row], ((u64)t5 << 32) | t4);
    atomicAdd(&sh.acc[3][row], ((u64)t7 << 32) | t6);
    atomicAdd(&sh.deg[row], 1u);
}
__device__ __forceinline__ void accumulate_wide(double* __restrict__ wide, u32 row, u32 angbits, u32x2 mm, float fr0, float fr1) {
    if (row >= (u32)EPB) return;
    const float a = __uint_as_float(angbits);
    const float c0 = __builtin_amdgcn_cosf(__builtin_amdgcn_fractf(a * fr0));
    const float c1 = __builtin_amdgcn_cosf(__builtin_amdgcn_fractf(a * fr1));
    const float m[4] = {__uint_as_float(mm.x << 16), __uint_as_float(mm.x & 0xffff0000u), __uint_as_float(mm.y << 16), __uint_as_float(mm.y & 0xffff0000u)};
#pragma unroll
    for (int k = 0; k < 4; k++) {
        atomicAdd(&wide[row * 8 + k], (double)(c0 * m[k]));
        atomicAdd(&wide[row * 8 + 4 + k], (double)(c1 * m[k]));
    }
}

__global__ __launch_bounds__(1024, 4) void p2_kernel(const u32* __restrict__ rec, const u32* __restrict__ tab,
                                                     const u32* __restrict__ mtab, const u16* __restrict__ dist,
                                                     const u16* __restrict__ centers, const u16* __restrict__ ang_freq,
                                                     const u16* __restrict__ w_rbf, const u16* __restrict__ w_sbf,
                                                     const u16* __restrict__ w_down, const u16* __restrict__ w_up,
                                                     double* __restrict__ wide_all, u32* __restrict__ out) {
    __shared__ __attribute__((aligned(16))) P2Shared sh;
    int t = threadIdx.x;
    const int lane = t & 63;
    const int wave = __builtin_amdgcn_readfirstlane(t >> 6);
    const int bin = (blockIdx.x & 7) * (NBIN / 8) + (blockIdx.x >> 3);
    const __amdgpu_buffer_rsrc_t r_rec = rsrc(rec, (u32)WS_REC_BYTES), r_tab = rsrc(tab, (u32)WS_TAB_BYTES);
    const __amdgpu_buffer_rsrc_t r_m = rsrc(mtab, E_ * 8), r_dist = rsrc(dist, E_ * 2), r_res = rsrc(out, E_ * 8);
    double* wide = wide_all + (size_t)bin * EPB * 8;

    u32x2 e0, e1;
    auto load_table = [&]() {
        const u32 b = t / CPB, ci = t - b * CPB;
        const u32 off = t < NCH ? b * ROWB + (bin * CPB + ci) * 8 : OOB - CPB * 8;
        e0 = __builtin_amdgcn_raw_buffer_load_b64(r_tab, off, 0, 0);
        e1 = __builtin_amdgcn_raw_buffer_load_b64(r_tab, off + CPB * 8, 0, 0);
    };
    load_table();
    auto edge = [&](u32 el) -> u32 { return ((el >> 4) << 12) | (bin << 4) | (el & 15); };
    u32 dv4[4];
#pragma unroll
    for (int i = 0; i < 4; i++) dv4[i] = __builtin_amdgcn_raw_buffer_load_b16(r_dist, (t + 1024 * i < EPB) ? edge(t + 1024 * i) * 2 : OOB, 0, 0);
    auto wload = [&](const u16* p, int first, int n) -> u32 { return __builtin_amdgcn_raw_buffer_load_b16(rsrc(p, n * 2), (u32)(t - first) * 2, 0, 0); };
    const u32 wv0 = wload(w_sbf, 0, 64), wv1 = wload(w_rbf, 64, 32), wv2 = wload(w_down, 96, 16), wv3 = wload(w_up, 112, 16), wv4 = wload(centers, 128, 8);
    const u32 fbits = *(const u32*)ang_freq;
    const float fr0 = __uint_as_float(fbits << 16) * 0.15915494309189535f, fr1 = __uint_as_float(fbits & 0xffff0000u) * 0.15915494309189535f;
    __builtin_amdgcn_sched_barrier(0);
    for (int i = t; i < 4 * ACCN / 2; i += 1024) ((u32x4*)sh.acc)[i] = (u32x4){0, 0, 0, 0};
    for (int i = t; i < ACCN; i += 1024) sh.deg[i] = 0;
    if (t < WIN / 64) *(u32x4*)&sh.desc[t] = (u32x4){0, 0, 0, 0};
    if (t < 3) sh.misc[t] = t == 2 ? 32 : 0;
    __builtin_amdgcn_sched_barrier(0);
    if (t < 136) sh.w[t] = bf2f(wv0 | wv1 | wv2 | wv3 | wv4);
    {
        const u32 mx = wave_max((e0.y >> 16) & 0xff), mn = wave_max(e0.y >> 24);
        if (lane == 63) { sh.wmax[0][wave] = mx; sh.wmax[1][wave] = mn; }
    }

    bool slow = false;
    float S = 1.0f, invS = 1.0f;
    bool unfit = false;
    u32 win = 0;
#pragma unroll 1
    for (;;) {
        asm volatile("" : "+v"(t));
        u32 len[NR], st[NR], inc[NR];
        st[0] = e0.x & 0xffff; st[1] = e0.x >> 16; st[2] = e0.y & 0xffff;
        len[0] = (e1.x & 0xffff) - st[0]; len[1] = (e1.x >> 16) - st[1]; len[2] = (e1.y & 0xffff) - st[2];
        u32 ninc = 0, ntot = 0;
#pragma unroll
        for (int s = 0; s < NR; s++) {
            inc[s] = wave_scan(len[s]);
            const u64 ne = __ballot(len[s] != 0);
            ninc |= __builtin_amdgcn_mbcnt_hi((u32)(ne >> 32), __builtin_amdgcn_mbcnt_lo((u32)ne, 0)) << (10 * s);
            ntot |= (u32)__popcll(ne) << (10 * s);
            const u32 wt = __builtin_amdgcn_readlane(inc[s], 63);
            if (lane == 0) sh.wsum[s][wave] = wt;
        }
        if (lane == 0) sh.wsum[NR][wave] = ntot;
        u32 warm = 0;
        if (win == 0 && !slow) {
            constexpr u32 lines = (R1 * 8 / 128 + 31) / 32;
            warm = __builtin_amdgcn_raw_buffer_load_b32(r_m, (u32)t < lines ? ((blockIdx.x >> 3) * lines + t) * 128 : OOB, 0, 0);
        }
        __syncthreads();
        if (win == 0 && !slow) {
            const u32 emax = __builtin_amdgcn_readlane(wave_max(lane < 16 ? sh.wmax[0][lane] : 0), 63);
            const u32 emin = 255 - __builtin_amdgcn_readlane(wave_max(lane < 16 ? sh.wmax[1][lane] : 0), 63);
            const int ex = max((int)emax - 127 + 1, -100);
            S = __uint_as_float((u32)(22 - ex + 127) << 23);
            invS = __uint_as_float((u32)(127 - 22 + ex) << 23);
            unfit = emax == 255 || (emin != 255 && ((int)emax - (int)emin > 13 || emax < 27)) ||
                    (fbits & 0x7f80u) == 0x7f80u || (fbits & 0x7f800000u) == 0x7f800000u;
        }
        const u32 W0 = win * WIN;
        u32 xs[NR + 1], sc[NR + 1];
#pragma unroll
        for (int s = 0; s <= NR; s++) {
            xs[s] = lane < 16 ? sh.wsum[s][lane] : 0;
            sc[s] = row_scan(xs[s]);
        }
        const u32 npre = __builtin_amdgcn_readlane(sc[NR] - xs[NR], wave), nall = __builtin_amdgcn_readlane(sc[NR], 15);
        u32 L = 0, nseg = 0;
#pragma unroll
        for (int s = 0; s < NR; s++) {
            const u32 P = L + __builtin_amdgcn_readlane(sc[s] - xs[s], wave) + inc[s] - len[s];
            const u32 ord = nseg + ((npre >> (10 * s)) & 1023) + ((ninc >> (10 * s)) & 1023);
            L += __builtin_amdgcn_readlane(sc[s], 15);
            nseg += (nall >> (10 * s)) & 1023;
            if (len[s]) {
                sh.delta[ord] = (u32)t * NU + st[s] - P;
                const u32 p = P - W0;
                if (p < WIN) atomicOr((p & 32) ? &sh.desc[p >> 6].hi : &sh.desc[p >> 6].lo, 1u << (p & 31));
                const u32 end = P + len[s];
                if (end >= W0 && P < W0 + WIN) {
                    u32 w = P < W0 ? 0 : (p >> 6) + 1;
                    const u32 w1 = min((end - W0) >> 6, (u32)(WIN / 64 - 1));
                    for (; w <= w1; w++) sh.desc[w].base = ord + 1;
                }
            }
        }
        const u32 nwin = (L + WIN - 1) / WIN;
        __syncthreads();

        const u32 rem = L > W0 ? min(L - W0, (u32)WIN) : 0;
        const u32 nit = (rem + 63) >> 6;

        auto fetch = [&](u32 k) -> u32x3 {
            const u32x4 d = *(const u32x4*)&sh.desc[min(k, (u32)(WIN / 64 - 1))];
            const u32 lo = __builtin_amdgcn_readfirstlane(d.x), hi = __builtin_amdgcn_readfirstlane(d.y), b = __builtin_amdgcn_readfirstlane(d.z);
            const u64 w1 = (((u64)hi << 32) | lo) >> 1;
            const u32 s = b + (lo & 1) - 1 + __builtin_amdgcn_mbcnt_hi((u32)(w1 >> 32), __builtin_amdgcn_mbcnt_lo((u32)w1, 0));
            const u32 i = k * 64 + lane;
            const bool ok = i < rem;
            const u32 dd = sh.delta[ok ? s : 0];
            return __builtin_amdgcn_raw_buffer_load_b96(r_rec, ok ? (dd + W0 + i) * 12 : OOB, 0, 0);
        };
        auto gather = [&](const u32x3& r, u32 k, u32x2& g0, u32x2& g1) {
            const bool ok = k * 64 + lane < rem;
            g0 = __builtin_amdgcn_raw_buffer_load_b64(r_m, ok ? (r.x >> 12) * 8 : OOB, 0, 0);
            g1 = __builtin_amdgcn_raw_buffer_load_b64(r_m, ok ? (r.y >> 12) * 8 : OOB, 0, 0);
        };
        auto ticket = [&]() -> u32 { u32 v = 0; if (lane == 0) v = atomicAdd(&sh.misc[2], 1u); return v; };

        float G[3][8], rw[3][4];
        u32x2 mm[3];
        auto edge_terms = [&](auto NEc, auto I0c, auto& Go, auto& rwo) {
            constexpr int NE = decltype(NEc)::value, I0 = decltype(I0c)::value;
#pragma unroll
            for (int i = 0; i < NE; i++) {
#pragma unroll
                for (int q = 0; q < 8; q++) Go[i][q] = 0;
#pragma unroll
                for (int q = 0; q < 4; q++) rwo[i][q] = 0;
            }
#pragma unroll 1
            for (int r = 0; r < 8; r++) {
                const f32x4 w0 = *(const f32x4*)&sh.w[r * 8], w1 = *(const f32x4*)&sh.w[r * 8 + 4], w2 = *(const f32x4*)&sh.w[64 + r * 4];
                const float cr = sh.w[128 + r];
#pragma unroll
                for (int i = 0; i < NE; i++) {
                    const float x = bf2f(dv4[I0 + i]) - cr;
                    const float rb = __builtin_amdgcn_exp2f(-14.426950408889634f * x * x);
#pragma unroll
                    for (int q = 0; q < 4; q++) { Go[i][q] += rb * w0[q]; Go[i][4 + q] += rb * w1[q]; rwo[i][q] += rb * w2[q]; }
                }
            }
        };
        auto all_edge_terms = [&]() {
            if (wave == 0) {
                float Gx[1][8], rwx[1][4];
                edge_terms(std::integral_constant<int, 1>{}, std::integral_constant<int, 3>{}, Gx, rwx);
                *(f32x4*)&sh.xg[0][lane][0] = (f32x4){Gx[0][0], Gx[0][1], Gx[0][2], Gx[0][3]};
                *(f32x4*)&sh.xg[1][lane][0] = (f32x4){Gx[0][4], Gx[0][5], Gx[0][6], Gx[0][7]};
                *(f32x4*)&sh.xg[2][lane][0] = (f32x4){rwx[0][0], rwx[0][1], rwx[0][2], rwx[0][3]};
            }
            edge_terms(std::integral_constant<int, 3>{}, std::integral_constant<int, 0>{}, G, rw);
        };
        auto finish = [&](auto NEc, auto I0c, bool wide_sums) -> bool {
            constexpr int NE = decltype(NEc)::value, I0 = decltype(I0c)::value;
            bool bad = false;
#pragma unroll
            for (int i = 0; i < NE; i++) {
                asm volatile("" ::: "memory");
                const int el = t + 1024 * (I0 + i);
                float Aa[8];
                if (wide_sums) {
#pragma unroll
                    for (int q = 0; q < 8; q++) Aa[q] = (float)__hip_atomic_load(&wide[el * 8 + q], __ATOMIC_RELAXED, __HIP_MEMORY_SCOPE_AGENT);
                } else {
                    const u32 dg = sh.deg[el];
                    bad |= dg > MAXDEG;
                    const u64 kk = (u64)dg * 0x4B4000004B400000ull;
#pragma unroll
                    for (int q = 0; q < 4; q++) {
                        const u64 v = sh.acc[q][el] - kk;
                        const int lo = (int)(u32)v;
                        const int hi = (int)((u32)(v >> 32) - (u32)(lo >> 31));
                        Aa[2 * q] = (float)lo * invS;
                        Aa[2 * q + 1] = (float)hi * invS;
                    }
                }
                f32x4 b0 = {0, 0, 0, 0}, b1 = {0, 0, 0, 0}, u = {0, 0, 0, 0};
#pragma unroll
                for (int k = 0; k < 4; k++) { const f32x4 wd = *(const f32x4*)&sh.w[96 + k * 4]; b0 += Aa[k] * wd; b1 += Aa[4 + k] * wd; }
#pragma unroll
                for (int k = 0; k < 4; k++) u += (G[i][k] * b0[k] + G[i][4 + k] * b1[k]) * *(const f32x4*)&sh.w[112 + k * 4];
                const float o0 = __uint_as_float(mm[i].x << 16) + u[0] * rw[i][0], o1 = __uint_as_float(mm[i].x & 0xffff0000u) + u[1] * rw[i][1];
                const float o2 = __uint_as_float(mm[i].y << 16) + u[2] * rw[i][2], o3 = __uint_as_float(mm[i].y & 0xffff0000u) + u[3] * rw[i][3];
                __builtin_amdgcn_raw_buffer_store_b64((u32x2){pack_bf16(o0, o1), pack_bf16(o2, o3)}, r_res, edge(el) * 8, 0, 0);
            }
            return bad;
        };

        auto mainloop = [&](auto SLOWc) {
            constexpr bool SLOW = decltype(SLOWc)::value;
            u32x3 cur, nxt;
            u32 kc = wave, kn = wave + 16, tk = 0;
            const bool any = kc < nit;
            if (any) {
                tk = ticket();
                cur = fetch(kc);
                nxt = fetch(kn);
            }
            __builtin_amdgcn_sched_barrier(0);
            all_edge_terms();
            asm volatile("" :: "v"(warm));
            __builtin_amdgcn_sched_barrier(0);
            if (any) {
                u32x2 c0, c1;
                gather(cur, kc, c0, c1);
                while (kc < nit) {
                    u32x2 n0, n1;
                    gather(nxt, kn, n0, n1);
                    const u32 knn = __builtin_amdgcn_readfirstlane(tk);
                    tk = ticket();
                    const u32x3 nn = fetch(knn);
                    __builtin_amdgcn_sched_barrier(0);
                    if (kc * 64 + lane < rem) {
                        if (SLOW) {
                            accumulate_wide(wide, cur.x & 0xfff, cur.z << 16, c0, fr0, fr1);
                            accumulate_wide(wide, cur.y & 0xfff, cur.z & 0xffff0000u, c1, fr0, fr1);
                        } else {
                            accumulate(sh, cur.x & 0xfff, cur.z << 16, c0, fr0, fr1, S);
                            accumulate(sh, cur.y & 0xfff, cur.z & 0xffff0000u, c1, fr0, fr1, S);
                        }
                    }
                    __builtin_amdgcn_sched_barrier(0);
                    cur = nxt; nxt = nn; c0 = n0; c1 = n1; kc = kn; kn = knn;
                }
            }
        };
        if (slow) { mainloop(std::true_type{}); asm volatile("s_waitcnt vmcnt(0)" ::: "memory"); }
        else mainloop(std::false_type{});

        win++;
        if (win >= nwin) {
#pragma unroll
            for (int i = 0; i < 3; i++) mm[i] = __builtin_amdgcn_raw_buffer_load_b64(r_m, edge(t + 1024 * i) * 8, 0, 0);
            const u32x2 mmx = __builtin_amdgcn_raw_buffer_load_b64(r_m, t < EPB - 3072 ? edge(t + 3072) * 8 : OOB, 0, 0);
            __syncthreads();
            if (slow) __threadfence();
            bool bad = finish(std::integral_constant<int, 3>{}, std::integral_constant<int, 0>{}, slow);
            if (wave == 0) {
                const f32x4 g0 = *(const f32x4*)&sh.xg[0][lane][0], g1 = *(const f32x4*)&sh.xg[1][lane][0], g2 = *(const f32x4*)&sh.xg[2][lane][0];
#pragma unroll
                for (int q = 0; q < 4; q++) { G[0][q] = g0[q]; G[0][4 + q] = g1[q]; rw[0][q] = g2[q]; }
                mm[0] = mmx;
                bad |= finish(std::integral_constant<int, 1>{}, std::integral_constant<int, 3>{}, slow);
            }
            if (slow) break;
            if (bad || unfit) sh.misc[1] = 1;
            __syncthreads();
            if (!sh.misc[1]) break;
            slow = true;
            win = 0;
            for (int i = t; i < EPB * 8; i += 1024) __hip_atomic_store(&wide[i], 0.0, __ATOMIC_RELAXED, __HIP_MEMORY_SCOPE_AGENT);
            __threadfence();
        } else {
            __syncthreads();
        }
        load_table();
        if (t < WIN / 64) *(u32x4*)&sh.desc[t] = (u32x4){0, 0, 0, 0};
        if (t == 0) sh.misc[2] = 32;
        __syncthreads();
    }
}

extern "C" void kernel_launch(void* const* d_in, const int* in_sizes, int n_in, void* d_out, int out_size, void* d_ws, size_t ws_size,
                              hipStream_t stream) {
    const u32* m = (const u32*)d_in[0];
    const u32* idx_in = (const u32*)d_in[1];
    const u32* idx_out = (const u32*)d_in[2];
    const u16* dist = (const u16*)d_in[3];
    const u16* angle = (const u16*)d_in[4];
    const u16* centers = (const u16*)d_in[5];
    const u16* ang_freq = (const u16*)d_in[6];
    const u16* w_rbf = (const u16*)d_in[7];
    const u16* w_sbf = (const u16*)d_in[8];
    const u16* w_down = (const u16*)d_in[9];
    const u16* w_up = (const u16*)d_in[10];
    char* ws = (char*)d_ws;
    u32* rec = (u32*)(ws + WS_REC);
    u32* tab = (u32*)(ws + WS_TAB);
    p1_kernel<<<P1B, P1T, 0, stream>>>(idx_in, idx_out, angle, m, rec, tab);
    p2_kernel<<<NBIN, 1024, 0, stream>>>(rec, tab, m, dist, centers, ang_freq, w_rbf, w_sbf, w_down, w_up, (double*)(ws + WS_WIDE), (u32*)d_out);
}
```
